```python
import torch

def _softmax(x, axis=-1):
    return torch.softmax(x, dim=axis)
def _gelu(x):
    return torch.nn.functional.gelu(x, approximate="tanh")
def _silu(x):
    return torch.nn.functional.silu(x)
def _sigmoid(x):
    return torch.sigmoid(x)
def _layernorm(x, axis=-1, eps=1e-5):
    m = x.mean(dim=axis, keepdim=True)
    v = x.var(dim=axis, keepdim=True, unbiased=False)
    return (x - m) / torch.sqrt(v + eps)
def _scatter_add(tgt, idx, src, axis=0):
    return tgt.clone().index_add_(axis, idx, src)
def _segment_sum(src, seg, n):
    out = torch.zeros((n,) + src.shape[1:], dtype=src.dtype)
    return out.index_add_(0, seg, src)
def _pad(x, pw):
    flat = tuple(v for pair in reversed(pw) for v in pair)
    return torch.nn.functional.pad(x, flat)

kernel_name = 'standalone__paged_block_table_kv_pagedattention__sampling'

def setup_inputs():

    _w = (lambda a: (lambda t: t.to(torch.bfloat16) if t.is_floating_point() else t)(torch.as_tensor(a)))
    B, Hq, Hkv, Sq, Skv, D = 16, 32, 8, 16, 4096, 128
    bs = 16
    nbps = max(1, Skv // bs)
    num_phys = B * nbps + 4
    bt = torch.randperm(num_phys)[: B * nbps].reshape(B, nbps).to(torch.int64)
    cl = (nbps * bs - (torch.arange(1, B + 1, dtype=torch.int64) * 2654435761) % max(1, nbps * bs // 2)).to(torch.int64)
    return {
        "q": _w((lambda *s: torch.randn(*s, dtype=torch.bfloat16) * 0.8)(B, Hq, Sq, D)),
        "k_cache": _w((lambda *s: torch.randn(*s, dtype=torch.bfloat16) * 0.8)(num_phys, bs, Hkv, D)),
        "v_cache": _w((lambda *s: torch.randn(*s, dtype=torch.bfloat16) * 0.05)(num_phys, bs, Hkv, D)),
        "block_table": _w(bt),
        "context_lens": _w(cl),
    }

def reference(q, k_cache, v_cache, block_table, context_lens):
    B, Hq, Sq, D = q.shape
    bs = k_cache.shape[1]
    Hkv = k_cache.shape[2]
    nbps = block_table.shape[1]
    Skv = nbps * bs
    bt = block_table.reshape(-1)
    k = (lambda x, *d: x.permute(*d))(k_cache[bt].reshape(B, Skv, Hkv, D), 0, 2, 1, 3)
    v = (lambda x, *d: x.permute(*d))(v_cache[bt].reshape(B, Skv, Hkv, D), 0, 2, 1, 3)
    g = Hq // Hkv
    qg = q.reshape(B, Hkv, g, Sq, D)
    scores = torch.einsum("bhgqd,bhkd->bhgqk", qg, k) * (D ** -0.5)
    pos = torch.arange(Skv)
    msk = pos[None, :] < context_lens[:, None]
    scores = torch.where(msk[:, None, None, None, :], scores, -1e9)
    p = _softmax(scores, axis=-1)
    out = torch.einsum("bhgqk,bhkd->bhgqd", p, v)
    return out.reshape(B, Hq, Sq, D)
```

```cpp
#include <hip/hip_runtime.h>
#include <hip/hip_bf16.h>
#include <stdint.h>

typedef __bf16 bf16x8 __attribute__((ext_vector_type(8)));
typedef __bf16 bf16x4 __attribute__((ext_vector_type(4)));
typedef __bf16 bf16x2 __attribute__((ext_vector_type(2)));
typedef float f32x4 __attribute__((ext_vector_type(4)));
typedef float f32x2 __attribute__((ext_vector_type(2)));
typedef unsigned u32x4 __attribute__((ext_vector_type(4)));
typedef unsigned u32x2 __attribute__((ext_vector_type(2)));
#define LAS __attribute__((address_space(3)))
#define DI __device__ __forceinline__

constexpr int B_ = 16, HQ = 32, HKV = 8, SQ = 16, D_ = 128, PAGE = 16, NBPS = 256, SKV = NBPS * PAGE;
#define NSLOT 9
constexpr int DEPTH = NSLOT - 1;
constexpr int SLOT_BYTES = 16384;
#define THR 8.0f
#define CPOL "nt"
constexpr float SCALE_LOG2E = 0.08838834764831845f * 1.4426950408889634f;

DI unsigned cvtpk(float lo, float hi) {
  f32x2 v = {lo, hi};
  bf16x2 b = __builtin_convertvector(v, bf16x2);
  return __builtin_bit_cast(unsigned, b);
}
DI float fmax2(float a, float b) { return __builtin_elementwise_maximum(a, b); }
DI float fmax3(float a, float b, float c) { return fmax2(fmax2(a, b), c); }
DI float xg_max(float x) {
  auto s = __builtin_amdgcn_permlane16_swap(__float_as_uint(x), __float_as_uint(x), false, false);
  x = fmax2(__uint_as_float(s[0]), __uint_as_float(s[1]));
  auto t = __builtin_amdgcn_permlane32_swap(__float_as_uint(x), __float_as_uint(x), false, false);
  return fmax2(__uint_as_float(t[0]), __uint_as_float(t[1]));
}

DI void issue_tile(int pa, int pb, unsigned m0base, unsigned voffK, unsigned voffV, __amdgpu_buffer_rsrc_t srdK,
                   __amdgpu_buffer_rsrc_t srdV) {
  unsigned ta, tb, keep;
  asm volatile(
      "s_mov_b32 %2, m0\n\t"
      "s_lshl_b32 %0, %3, 15\n\t"
      "s_mov_b32 m0, %5\n\t"
      "s_lshl_b32 %1, %4, 15\n\t"
      "buffer_load_dwordx4 %6, %8, %0 offen " CPOL " lds\n\t"
      "s_add_u32 m0, m0, 0x1000\n\t"
      "s_nop 0\n\t"
      "buffer_load_dwordx4 %6, %8, %1 offen " CPOL " lds\n\t"
      "s_add_u32 m0, m0, 0x1000\n\t"
      "s_nop 0\n\t"
      "buffer_load_dwordx4 %7, %9, %0 offen " CPOL " lds\n\t"
      "s_add_u32 m0, m0, 0x1000\n\t"
      "s_nop 0\n\t"
      "buffer_load_dwordx4 %7, %9, %1 offen " CPOL " lds\n\t"
      "s_mov_b32 m0, %2"
      : "=&s"(ta), "=&s"(tb), "=&s"(keep)
      : "s"(pa), "s"(pb), "s"(m0base), "v"(voffK), "v"(voffV), "s"(srdK), "s"(srdV)
      : "memory", "scc");
}

template <int N> DI void wait_vm_barrier() {
  asm volatile("s_waitcnt vmcnt(%0) lgkmcnt(0)\n\ts_barrier" ::"i"(N) : "memory");
}

__global__ void __launch_bounds__(256, 2)
attn_main(const __bf16* __restrict__ q, const __bf16* __restrict__ kc, const __bf16* __restrict__ vc,
          const int* __restrict__ bt, const int* __restrict__ lens, float* __restrict__ part, unsigned kv_bytes) {
  extern __shared__ __attribute__((aligned(1024))) char lds[];
  const int tid = threadIdx.x, lane = tid & 63;
  const int w = __builtin_amdgcn_readfirstlane(tid >> 6);
  const int g = lane >> 4, i = lane & 15;
  const int h = blockIdx.x & 7, idx = blockIdx.x >> 3, b = idx >> 1, half = idx & 1;

  int len = lens[b];
  const bool zq = len <= 0;
  len = (len <= 0 || len > SKV) ? SKV : len;
  const int npages = (len + PAGE - 1) / PAGE;
  const int ntile = (len + 31) / 32;
  const int hn = (ntile + 1) / 2;
  const int t0 = half ? hn : 0, t1 = half ? ntile : hn;
  const int n = t1 - t0;

  int pA, pB;
  {
    int tj = t0 + lane;
    int ia = min(2 * tj, npages - 1), ib = min(2 * tj + 1, npages - 1);
    pA = bt[b * NBPS + ia];
    pB = bt[b * NBPS + ib];
  }
  bf16x8 qf[4];
  {
    const __bf16* qrow = q + ((size_t)(b * HQ + h * 4 + w) * SQ + i) * D_ + 8 * g;
#pragma unroll
    for (int s = 0; s < 4; ++s) {
      qf[s] = *(const bf16x8*)(qrow + 32 * s);
    }
  }
  asm volatile("" : "+v"(pA), "+v"(pB), "+v"(qf[0]), "+v"(qf[1]), "+v"(qf[2]), "+v"(qf[3]));
  const __amdgpu_buffer_rsrc_t srdK =
      __builtin_amdgcn_make_buffer_rsrc((void*)((const char*)kc + h * 256), (short)0, kv_bytes - h * 256, 0x00020000);
  const __amdgpu_buffer_rsrc_t srdV =
      __builtin_amdgcn_make_buffer_rsrc((void*)((const char*)vc + h * 256), (short)0, kv_bytes - h * 256, 0x00020000);
  const unsigned ldsbase = (unsigned)(size_t)lds;
  const int drow = 4 * w + g;
  const unsigned voffK = drow * 2048 + 16 * (i ^ drow);
  const unsigned voffV = drow * 2048 + 16 * (i ^ ((drow & 7) << 1));
  const unsigned m0w = ldsbase + w * 1024;
  const unsigned rdK = ldsbase + i * 256 + 16 * (g ^ i);
  const int vrow = 4 * g + (i >> 2);
  const unsigned rdV = ldsbase + 8192 + vrow * 256 + ((vrow & 7) << 5) + 8 * (i & 3);

  {
    const int np = n < DEPTH ? n : DEPTH;
    for (int t = 0; t < np; ++t)
      issue_tile(__builtin_amdgcn_readlane(pA, t), __builtin_amdgcn_readlane(pB, t), m0w + t * SLOT_BYTES, voffK, voffV,
                 srdK, srdV);
  }
  f32x4 acc[8];
#pragma unroll
  for (int e = 0; e < 8; ++e) acc[e] = f32x4{0.f, 0.f, 0.f, 0.f};
  f32x4 lacc = {0.f, 0.f, 0.f, 0.f};
  float m = -1e30f;
  bf16x8 ones;
#pragma unroll
  for (int j = 0; j < 8; ++j) ones[j] = (__bf16)1.0f;

  int slot = 0, islot = DEPTH % NSLOT;
  auto body = [&](int t, bool do_issue) __attribute__((always_inline)) {
    const unsigned so = slot * SLOT_BYTES;
    const unsigned ak = rdK + so, av = rdV + so;
    bf16x8 kA[4], kB[4];
#pragma unroll
    for (int s = 0; s < 4; ++s) {
      kA[s] = *(const LAS bf16x8*)(size_t)(ak ^ (s << 6));
      kB[s] = *(const LAS bf16x8*)(size_t)((ak ^ (s << 6)) + 4096);
    }
    if (do_issue)
      issue_tile(__builtin_amdgcn_readlane(pA, t + DEPTH), __builtin_amdgcn_readlane(pB, t + DEPTH),
                 m0w + islot * SLOT_BYTES, voffK, voffV, srdK, srdV);
    bf16x4 vA[8], vB[8];
#pragma unroll
    for (int e = 0; e < 8; ++e) {
      vA[e] = __builtin_amdgcn_ds_read_tr16_b64_v4bf16((LAS bf16x4*)(size_t)(av ^ (e << 5)));
      vB[e] = __builtin_amdgcn_ds_read_tr16_b64_v4bf16((LAS bf16x4*)(size_t)((av ^ (e << 5)) + 4096));
    }
    f32x4 sA = {0.f, 0.f, 0.f, 0.f}, sB = {0.f, 0.f, 0.f, 0.f};
#pragma unroll
    for (int s = 0; s < 4; ++s) {
      sA = __builtin_amdgcn_mfma_f32_16x16x32_bf16(kA[s], qf[s], sA, 0, 0, 0);
      sB = __builtin_amdgcn_mfma_f32_16x16x32_bf16(kB[s], qf[s], sB, 0, 0, 0);
    }
    const int kbase = (t0 + t) * 32;
    if (__builtin_expect(kbase + 32 > len || zq, 0)) {
      const int lim = len - kbase - 4 * g;
#pragma unroll
      for (int r = 0; r < 4; ++r) {
        sA[r] = zq ? 0.f : ((r < lim) ? sA[r] : -INFINITY);
        sB[r] = zq ? 0.f : ((r + 16 < lim) ? sB[r] : -INFINITY);
      }
    }
    float mx = fmax3(fmax3(sA[0], sA[1], sA[2]), fmax3(sA[3], sB[0], sB[1]), fmax2(sB[2], sB[3]));
    mx *= SCALE_LOG2E;
    if (__builtin_expect(__any(mx > m + THR), 0)) {
      mx = xg_max(mx);
      const float mn = fmax2(m, mx);
      const float alpha = __builtin_amdgcn_exp2f(m - mn);
      m = mn;
#pragma unroll
      for (int e = 0; e < 8; ++e) acc[e] *= alpha;
      lacc *= alpha;
    }
    float p[8];
#pragma unroll
    for (int r = 0; r < 4; ++r) {
      p[r] = __builtin_amdgcn_exp2f(fmaf(sA[r], SCALE_LOG2E, -m));
      p[4 + r] = __builtin_amdgcn_exp2f(fmaf(sB[r], SCALE_LOG2E, -m));
    }
    u32x4 pw = {cvtpk(p[0], p[1]), cvtpk(p[2], p[3]), cvtpk(p[4], p[5]), cvtpk(p[6], p[7])};
    const bf16x8 pf = __builtin_bit_cast(bf16x8, pw);
#pragma unroll
    for (int e = 0; e < 8; ++e) {
      const bf16x8 vf = __builtin_shufflevector(vA[e], vB[e], 0, 1, 2, 3, 4, 5, 6, 7);
      acc[e] = __builtin_amdgcn_mfma_f32_16x16x32_bf16(vf, pf, acc[e], 0, 0, 0);
    }
    lacc = __builtin_amdgcn_mfma_f32_16x16x32_bf16(ones, pf, lacc, 0, 0, 0);
    slot = (slot + 1 == NSLOT) ? 0 : slot + 1;
    islot = (islot + 1 == NSLOT) ? 0 : islot + 1;
  };
  int t = 0;
  for (; t + DEPTH < n; ++t) {
    wait_vm_barrier<4 * (DEPTH - 1)>();
    body(t, true);
  }
  for (; t < n; ++t) {
    const int rem = n - t;
    switch (rem) {
      case 2: wait_vm_barrier<4>(); break;
      case 3: wait_vm_barrier<8>(); break;
      case 4: wait_vm_barrier<12>(); break;
      case 5: wait_vm_barrier<16>(); break;
      case 6: wait_vm_barrier<20>(); break;
      case 7: wait_vm_barrier<24>(); break;
      case 8: wait_vm_barrier<28>(); break;
      case 9: wait_vm_barrier<32>(); break;
      default: wait_vm_barrier<0>(); break;
    }
    body(t, false);
  }
  f32x4* dst = (f32x4*)part + ((size_t)blockIdx.x * 4 + w) * 9 * 64 + lane;
#pragma unroll
  for (int e = 0; e < 8; ++e) dst[e * 64] = acc[e];
  dst[8 * 64] = f32x4{m, lacc[0], 0.f, 0.f};
}

__global__ void __launch_bounds__(256) attn_combine(const float* __restrict__ part, __bf16* __restrict__ out) {
  const int tid = threadIdx.x, lane = tid & 63, w = tid >> 6;
  const int g = lane >> 4, i = lane & 15;
  const int h = blockIdx.x & 7, b = blockIdx.x >> 3;
  const f32x4* p0 = (const f32x4*)part + ((size_t)(((2 * b) << 3) + h) * 4 + w) * 9 * 64 + lane;
  const f32x4* p1 = (const f32x4*)part + ((size_t)(((2 * b + 1) << 3) + h) * 4 + w) * 9 * 64 + lane;
  f32x4 a0[8], a1[8];
#pragma unroll
  for (int e = 0; e < 8; ++e) { a0[e] = p0[e * 64]; a1[e] = p1[e * 64]; }
  const f32x4 ml0 = p0[8 * 64], ml1 = p1[8 * 64];
  const float M = fmaxf(ml0[0], ml1[0]);
  const float w0 = __builtin_amdgcn_exp2f(ml0[0] - M), w1 = __builtin_amdgcn_exp2f(ml1[0] - M);
  const float inv = 1.0f / (w0 * ml0[1] + w1 * ml1[1]);
  const float c0 = w0 * inv, c1 = w1 * inv;
  __bf16* orow = out + ((size_t)(b * HQ + h * 4 + w) * SQ + i) * D_ + 4 * g;
#pragma unroll
  for (int e = 0; e < 8; ++e) {
    const f32x4 o = a0[e] * c0 + a1[e] * c1;
    u32x2 pk = {cvtpk(o[0], o[1]), cvtpk(o[2], o[3])};
    *(u32x2*)(orow + 16 * e) = pk;
  }
}

extern "C" void kernel_launch(void* const* d_in, const int* in_sizes, int n_in, void* d_out, int out_size, void* d_ws,
                              size_t ws_size, hipStream_t stream) {
  const __bf16* q = (const __bf16*)d_in[0];
  const __bf16* kc = (const __bf16*)d_in[1];
  const __bf16* vc = (const __bf16*)d_in[2];
  const int* bt = (const int*)d_in[3];
  const int* lens = (const int*)d_in[4];
  float* part = (float*)d_ws;
  const unsigned kv_bytes = (unsigned)in_sizes[1] * 2u;
  (void)hipFuncSetAttribute((const void*)attn_main, hipFuncAttributeMaxDynamicSharedMemorySize, NSLOT * SLOT_BYTES);
  attn_main<<<256, 256, NSLOT * SLOT_BYTES, stream>>>(q, kc, vc, bt, lens, part, kv_bytes);
  attn_combine<<<128, 256, 0, stream>>>(part, (__bf16*)d_out);
}
```

```cpp
#include <hip/hip_runtime.h>
#include <hip/hip_bf16.h>
#include <stdint.h>

typedef __bf16 bf16x8 __attribute__((ext_vector_type(8)));
typedef __bf16 bf16x4 __attribute__((ext_vector_type(4)));
typedef __bf16 bf16x2 __attribute__((ext_vector_type(2)));
typedef float f32x4 __attribute__((ext_vector_type(4)));
typedef float f32x2 __attribute__((ext_vector_type(2)));
typedef unsigned u32x4 __attribute__((ext_vector_type(4)));
typedef unsigned u32x2 __attribute__((ext_vector_type(2)));
typedef int i32x2 __attribute__((ext_vector_type(2)));
typedef unsigned long long u64;
#define LAS __attribute__((address_space(3)))
#define GAS __attribute__((address_space(1)))
#define DI __device__ __forceinline__
#define RLX_AGENT __ATOMIC_RELAXED, __HIP_MEMORY_SCOPE_AGENT

constexpr int HQ = 32, SQ = 16, D_ = 128, PAGE = 16, NBPS = 256, SKV = NBPS * PAGE;
constexpr int NSLOT = 8, SLOT_BYTES = 16384;
constexpr int BT_OFF = NSLOT * SLOT_BYTES;
constexpr int CTL_OFF = BT_OFF + 16384;
constexpr int LDS_BYTES = CTL_OFF + 512;
#define WIN 22
#define EXTRA 2
#define PUB_EARLY 2
#define PBF16 1
#define THR 8.0f
constexpr unsigned SPIN_CAP = 1u << 18;
constexpr float SCALE_LOG2E = 0.08838834764831845f * 1.4426950408889634f;
constexpr size_t WS_FLAG = 0, WS_PART = 8 * 4 * 32 * 16;
constexpr int NPK = PBF16 ? 4 : 8;
constexpr unsigned PART_BYTES = NPK * 1024 + 512;
constexpr u64 TAG_STEP = 0x9E3779B97F4A7C15ull;

#define STAMP(k)
#define STAMP_FLUSH(base, cnt)

DI unsigned cvtpk(float lo, float hi) {
  f32x2 v = {lo, hi};
  bf16x2 b = __builtin_convertvector(v, bf16x2);
  return __builtin_bit_cast(unsigned, b);
}
DI float fmax2(float a, float b) { return __builtin_elementwise_maximum(a, b); }
DI float fmax3(float a, float b, float c) { return fmax2(fmax2(a, b), c); }
DI float xg_max(float x) {
  auto s = __builtin_amdgcn_permlane16_swap(__float_as_uint(x), __float_as_uint(x), false, false);
  x = fmax2(__uint_as_float(s[0]), __uint_as_float(s[1]));
  auto t = __builtin_amdgcn_permlane32_swap(__float_as_uint(x), __float_as_uint(x), false, false);
  return fmax2(__uint_as_float(t[0]), __uint_as_float(t[1]));
}
DI int row_scan(int v) {
  v += __builtin_amdgcn_update_dpp(0, v, 0x111, 0xf, 0xf, true);
  v += __builtin_amdgcn_update_dpp(0, v, 0x112, 0xf, 0xf, true);
  v += __builtin_amdgcn_update_dpp(0, v, 0x114, 0xf, 0xf, true);
  v += __builtin_amdgcn_update_dpp(0, v, 0x118, 0xf, 0xf, true);
  return v;
}
DI int rl(int v, int l) { return __builtin_amdgcn_readlane(v, l); }

template <int W> DI void dma_piece(unsigned so, unsigned m0v, unsigned voff, __amdgpu_buffer_rsrc_t srd) {
  asm volatile("s_mov_b32 m0, %1\n\ts_waitcnt vmcnt(%4)\n\tbuffer_load_dwordx4 %2, %3, %0 offen nt lds"
               :: "s"(so), "s"(m0v), "v"(voff), "s"(srd), "i"(W - 1) : "memory");
}
DI void lds_store(unsigned addr, unsigned val) { asm volatile("ds_write_b32 %0, %1" :: "v"(addr), "v"(val) : "memory"); }

__global__ void __launch_bounds__(320, 2)
attn_paged(const __bf16* __restrict__ q, const __bf16* __restrict__ kc, const __bf16* __restrict__ vc,
           const int* __restrict__ bt, const int* __restrict__ lens, __bf16* __restrict__ out, char* ws, unsigned kv_bytes) {
  STAMP(0);
  const int tid = threadIdx.x, lane = tid & 63;
  const int w = __builtin_amdgcn_readfirstlane(tid >> 6);
  const int g = lane >> 4, i = lane & 15;
  const int h = blockIdx.x & 7, j = blockIdx.x >> 3;
  constexpr unsigned ldsbase = 0;

  const int rawlen = lens[i];
  if (w == 4) {
    lds_store(ldsbase + CTL_OFF + 4 * lane, 0u);
    lds_store(ldsbase + CTL_OFF + 256 + 4 * lane, 0u);
  } else {
    const __amdgpu_buffer_rsrc_t srdB = __builtin_amdgcn_make_buffer_rsrc((void*)bt, (short)0, 16 * NBPS * 4, 0x00020000);
    asm volatile("s_mov_b32 m0, %0\n\ts_nop 0\n\t"
                 "buffer_load_dwordx4 %1, %2, %3 offen lds\n\t"
                 "buffer_load_dwordx4 %1, %2, %3 offen offset:1024 lds\n\t"
                 "buffer_load_dwordx4 %1, %2, %3 offen offset:2048 lds\n\t"
                 "buffer_load_dwordx4 %1, %2, %3 offen offset:3072 lds"
                 :: "s"(ldsbase + BT_OFF + w * 4096), "v"(lane * 16), "s"(srdB), "s"(w * 4096) : "memory");
  }
  const int lenv = (rawlen <= 0 || rawlen > SKV) ? SKV : rawlen;
  const int nb = (lenv + 31) >> 5;
  const int nr = max(nb - EXTRA, 1);
  const int Pin = row_scan(nr), P = Pin - nr;
  const int R = rl(Pin, 15);
  const int lo = (j * R) >> 5, hi = ((j + 1) * R) >> 5;
  const int a0 = max(lo, P);
  const int cred = min(hi, Pin) - a0;
  const bool starts = (a0 == P);
  const int sfirst = starts ? 0 : (nb - nr) + a0 - P;
  const int scnt = cred + (starts ? nb - nr : 0);
  const unsigned segmask = (unsigned)__ballot(cred > 0) & 0xffffu;
  asm volatile("s_waitcnt vmcnt(0) lgkmcnt(0)\n\ts_barrier" ::: "memory");

  if (w == 4) {
    __builtin_amdgcn_s_setprio(3);
    int n = 0;
    int off0 = 0, off1 = 0, last0 = 0, last1 = 0;
    for (unsigned m = segmask; m; m &= m - 1) {
      const int b = __builtin_ctz(m);
      const int f = rl(sfirst, b) - n, lp = b * NBPS + ((rl(lenv, b) + 15) >> 4) - 1;
      if (lane >= n) { off0 = b * NBPS + 2 * min(f + lane, 127); last0 = lp; }
      if (lane + 64 >= n) { off1 = b * NBPS + 2 * min(f + lane + 64, 127); last1 = lp; }
      n += rl(scnt, b);
    }
    const i32x2 pg0 = *(const LAS i32x2*)(size_t)(ldsbase + BT_OFF + 4 * off0);
    const i32x2 pg1 = *(const LAS i32x2*)(size_t)(ldsbase + BT_OFF + 4 * off1);
    unsigned oA = (unsigned)pg0[0] << 15, oB = (unsigned)(off0 < last0 ? pg0[1] : pg0[0]) << 15;
    const unsigned oA1 = (unsigned)pg1[0] << 15, oB1 = (unsigned)(off1 < last1 ? pg1[1] : pg1[0]) << 15;
    const __amdgpu_buffer_rsrc_t srdK =
        __builtin_amdgcn_make_buffer_rsrc((void*)((const char*)kc + h * 256), (short)0, kv_bytes - h * 256, 0x00020000);
    const __amdgpu_buffer_rsrc_t srdV =
        __builtin_amdgcn_make_buffer_rsrc((void*)((const char*)vc + h * 256), (short)0, kv_bytes - h * 256, 0x00020000);
    unsigned vK[4], vV[2];
#pragma unroll
    for (int p = 0; p < 4; ++p) vK[p] = (4 * p + g) * 2048 + 16 * (i ^ (4 * p + g));
#pragma unroll
    for (int p = 0; p < 2; ++p) vV[p] = (4 * p + g) * 2048 + 16 * (i ^ ((4 * p + g) << 1));
    const unsigned fulladdr = ldsbase + CTL_OFF + 4 * lane;
    STAMP(1);
    int freed = 0;
    for (int t = 0; t < n; ++t) {
      if (t == 64) { oA = oA1; oB = oB1; }
      unsigned a = rl(oA, t & 63), b = rl(oB, t & 63);
      for (unsigned spins = 0; freed < t - NSLOT + 1 && spins < SPIN_CAP; ++spins) {
        u32x4 d;
        asm volatile("ds_read_b128 %0, %1\n\ts_waitcnt lgkmcnt(0)" : "=v"(d) : "v"(ldsbase + CTL_OFF + 256) : "memory");
        freed = __builtin_amdgcn_readfirstlane(min(min(d[0], d[1]), min(d[2], d[3])));
      }
      asm volatile("s_nop 4" : "+s"(a), "+s"(b));
      const unsigned mb = ldsbase + (t & (NSLOT - 1)) * SLOT_BYTES;
#pragma unroll
      for (int u = 0; u < 16; ++u) {
        const unsigned so = ((u & 4) ? b : a) + ((u & 8) ? (u & 2) * 8192 : 0);
        dma_piece<WIN>(so, mb + u * 1024, (u & 8) ? vV[u & 1] : vK[u & 3], (u & 8) ? srdV : srdK);
        if (((u + 1 - WIN) & 7) == 0) lds_store(fulladdr, (unsigned)max((16 * t + u + 1 - WIN) >> 3, 0));
      }
    }
    STAMP(2);
    if (WIN > 24) { asm volatile("s_waitcnt vmcnt(24)" ::: "memory"); lds_store(fulladdr, (unsigned)max(2 * n - 3, 0)); }
    asm volatile("s_waitcnt vmcnt(16)" ::: "memory");
    lds_store(fulladdr, (unsigned)max(2 * n - 2, 0));
    asm volatile("s_waitcnt vmcnt(8)" ::: "memory");
    lds_store(fulladdr, (unsigned)(2 * n - 1));
    asm volatile("s_waitcnt vmcnt(0)" ::: "memory");
    lds_store(fulladdr, (unsigned)(2 * n));
    STAMP(3);
    STAMP_FLUSH(0, 8);
    return;
  }

  if (segmask == 0) return;
  const unsigned rdK = ldsbase + i * 256 + 16 * (g ^ i);
  const int vrow = 4 * g + (i >> 2);
  const unsigned rdV = ldsbase + 8192 + vrow * 256 + ((vrow & 7) << 5) + 8 * (i & 3);
  const unsigned doneaddr = ldsbase + CTL_OFF + 256 + 4 * w;
  volatile LAS unsigned* fullp = (volatile LAS unsigned*)(size_t)(ldsbase + CTL_OFF);
  bf16x8 ones;
#pragma unroll
  for (int k = 0; k < 8; ++k) ones[k] = (__bf16)1.0f;
  const __amdgpu_buffer_rsrc_t srdP = __builtin_amdgcn_make_buffer_rsrc((void*)(ws + WS_PART), (short)0, 8 * 32 * 4 * PART_BYTES, 0x00020000);
  GAS u64* ef = (GAS u64*)(ws + WS_FLAG) + (h * 4 + w) * 64;
  const u64 mytag = __hip_atomic_load(ef + 2 * j, RLX_AGENT) + TAG_STEP;
  const __bf16* qbase = q + ((size_t)(h * 4 + w) * SQ + i) * D_ + 8 * g;
  const int ntot = rl(row_scan(cred > 0 ? scnt : 0), 15);

  bf16x8 qn[4];
#pragma unroll
  for (int s = 0; s < 4; ++s) qn[s] = *(const bf16x8*)(qbase + (size_t)__builtin_ctz(segmask) * HQ * SQ * D_ + 32 * s);

  u32x4 park[NPK];
  f32x2 parkml = {0.f, 0.f};
#pragma unroll
  for (int e = 0; e < NPK; ++e) park[e] = u32x4{0u, 0u, 0u, 0u};
  int pstate = 0;
  const unsigned pbase = ((h * 32 + j) * 4 + w) * PART_BYTES;
  auto publish = [&]() __attribute__((always_inline)) {
#pragma unroll
    for (int e = 0; e < NPK; ++e) __builtin_amdgcn_raw_buffer_store_b128(park[e], srdP, pbase + lane * 16 + e * 1024, 0, 16);
    __builtin_amdgcn_raw_buffer_store_b64(__builtin_bit_cast(u32x2, parkml), srdP, pbase + NPK * 1024 + lane * 8, 0, 16);
    pstate = 2;
  };

  int tg = 0;
  unsigned full = 0;
  for (unsigned m = segmask; m;) {
    const int b = __builtin_ctz(m);
    m &= m - 1;
    const int first = rl(sfirst, b), cnt = rl(scnt, b), len = rl(lenv, b);
    const bool zq = rl(rawlen, b) <= 0;
    bf16x8 qf[4];
#pragma unroll
    for (int s = 0; s < 4; ++s) qf[s] = qn[s];
    asm volatile("" : "+v"(qf[0]), "+v"(qf[1]), "+v"(qf[2]), "+v"(qf[3]));
    {
      const int bn = m ? __builtin_ctz(m) : b;
#pragma unroll
      for (int s = 0; s < 4; ++s) qn[s] = *(const bf16x8*)(qbase + (size_t)bn * HQ * SQ * D_ + 32 * s);
    }
    f32x4 acc[8];
#pragma unroll
    for (int e = 0; e < 8; ++e) acc[e] = f32x4{0.f, 0.f, 0.f, 0.f};
    f32x4 lacc = {0.f, 0.f, 0.f, 0.f};
    float mrun = -1e30f;

    for (int t = 0; t < cnt; ++t, ++tg) {
      if (__builtin_expect(pstate == 1 && tg + PUB_EARLY >= ntot, 0)) publish();
      for (unsigned spins = 0; full < (unsigned)(2 * tg + 1) && spins < SPIN_CAP; ++spins) full = __builtin_amdgcn_readfirstlane(*fullp);
      asm volatile("" ::: "memory");
      if (tg == 0) STAMP(1);
      const unsigned so = (tg & (NSLOT - 1)) * SLOT_BYTES;
      const unsigned ak = rdK + so, av = rdV + so;
      bf16x8 kA[4], kB[4];
#pragma unroll
      for (int s = 0; s < 4; ++s) {
        kA[s] = *(const LAS bf16x8*)(size_t)(ak ^ (s << 6));
        kB[s] = *(const LAS bf16x8*)(size_t)((ak ^ (s << 6)) + 4096);
      }
      f32x4 sA = {0.f, 0.f, 0.f, 0.f}, sB = {0.f, 0.f, 0.f, 0.f};
#pragma unroll
      for (int s = 0; s < 4; ++s) {
        sA = __builtin_amdgcn_mfma_f32_16x16x32_bf16(kA[s], qf[s], sA, 0, 0, 0);
        sB = __builtin_amdgcn_mfma_f32_16x16x32_bf16(kB[s], qf[s], sB, 0, 0, 0);
      }
      const int kbase = (first + t) * 32;
      if (__builtin_expect(kbase + 32 > len || zq, 0)) {
        const int lim = len - kbase - 4 * g;
#pragma unroll
        for (int r = 0; r < 4; ++r) {
          sA[r] = zq ? 0.f : ((r < lim) ? sA[r] : -INFINITY);
          sB[r] = zq ? 0.f : ((r + 16 < lim) ? sB[r] : -INFINITY);
        }
      }
      float mx = fmax3(fmax3(sA[0], sA[1], sA[2]), fmax3(sA[3], sB[0], sB[1]), fmax2(sB[2], sB[3]));
      mx *= SCALE_LOG2E;
      if (__builtin_expect(__any(mx > mrun + THR), 0)) {
        mx = xg_max(mx);
        const float mn = fmax2(mrun, mx);
        const float alpha = __builtin_amdgcn_exp2f(mrun - mn);
        mrun = mn;
#pragma unroll
        for (int e = 0; e < 8; ++e) acc[e] *= alpha;
        lacc *= alpha;
      }
      float p[8];
#pragma unroll
      for (int r = 0; r < 4; ++r) {
        p[r] = __builtin_amdgcn_exp2f(fmaf(sA[r], SCALE_LOG2E, -mrun));
        p[4 + r] = __builtin_amdgcn_exp2f(fmaf(sB[r], SCALE_LOG2E, -mrun));
      }
      u32x4 pw = {cvtpk(p[0], p[1]), cvtpk(p[2], p[3]), cvtpk(p[4], p[5]), cvtpk(p[6], p[7])};
      asm volatile("" : "+v"(pw));
      const bf16x8 pf = __builtin_bit_cast(bf16x8, pw);
      for (unsigned spins = 0; full < (unsigned)(2 * tg + 2) && spins < SPIN_CAP; ++spins) full = __builtin_amdgcn_readfirstlane(*fullp);
      asm volatile("" ::: "memory");
      bf16x4 vA[8], vB[8];
#pragma unroll
      for (int e = 0; e < 8; ++e) {
        vA[e] = __builtin_amdgcn_ds_read_tr16_b64_v4bf16((LAS bf16x4*)(size_t)(av ^ (e << 5)));
        vB[e] = __builtin_amdgcn_ds_read_tr16_b64_v4bf16((LAS bf16x4*)(size_t)((av ^ (e << 5)) + 4096));
      }
      lds_store(doneaddr, (unsigned)(tg + 1));
      lacc = __builtin_amdgcn_mfma_f32_16x16x32_bf16(ones, pf, lacc, 0, 0, 0);
#pragma unroll
      for (int e = 0; e < 8; ++e) {
        const bf16x8 vf = __builtin_shufflevector(vA[e], vB[e], 0, 1, 2, 3, 4, 5, 6, 7);
        acc[e] = __builtin_amdgcn_mfma_f32_16x16x32_bf16(vf, pf, acc[e], 0, 0, 0);
      }
    }

    float lrun = lacc[0];
    if (first != 0) {
      if (PBF16) {
        const float inv = __builtin_amdgcn_rcpf(lrun);
#pragma unroll
        for (int e = 0; e < 4; ++e) {
          const f32x4 x = acc[2 * e] * inv, y = acc[2 * e + 1] * inv;
          park[e] = u32x4{cvtpk(x[0], x[1]), cvtpk(x[2], x[3]), cvtpk(y[0], y[1]), cvtpk(y[2], y[3])};
        }
      } else {
#pragma unroll
        for (int e = 0; e < NPK; ++e) park[e] = __builtin_bit_cast(u32x4, acc[e & 7]);
      }
      parkml = f32x2{mrun, lrun};
      pstate = 1;
      if (m) continue;
    }
    if (!m) {
      STAMP(2);
      if (pstate == 1) publish();
      if (pstate == 2) {
        asm volatile("s_waitcnt vmcnt(0)" ::: "memory");
        if (lane == 0) __hip_atomic_store(ef + 2 * j + 1, mytag, RLX_AGENT);
      }
      STAMP(3);
      if (first != 0) break;
      const int Pe = rl(Pin, b);
      const int k = lane & 31;
      const bool mine = k > j && ((k * R) >> 5) < Pe && (((k + 1) * R) >> 5) > ((k * R) >> 5);
      const unsigned hmask = (unsigned)__ballot(mine);
      if (hmask) {
        u64 e0 = 0;
        for (unsigned spins = 0; spins < SPIN_CAP; ++spins) {
          e0 = __hip_atomic_load(ef + 2 * k, RLX_AGENT);
          const u64 f0 = __hip_atomic_load(ef + 2 * k + 1, RLX_AGENT);
          if (__all(!mine || f0 == e0 + TAG_STEP)) break;
          __builtin_amdgcn_s_sleep(1);
        }
        asm volatile("" ::: "memory");
        STAMP(4);
#pragma unroll 1
        for (unsigned hm = hmask; hm; hm &= hm - 1) {
          const unsigned base = ((h * 32 + __builtin_ctz(hm)) * 4 + w) * PART_BYTES;
          u32x4 pa[NPK];
          const f32x2 ml = __builtin_bit_cast(f32x2, __builtin_amdgcn_raw_buffer_load_b64(srdP, base + NPK * 1024 + lane * 8, 0, 16));
#pragma unroll
          for (int e = 0; e < NPK; ++e) pa[e] = __builtin_amdgcn_raw_buffer_load_b128(srdP, base + lane * 16 + e * 1024, 0, 16);
          const float mn = fmax2(mrun, ml[0]);
          const float c0 = __builtin_amdgcn_exp2f(mrun - mn);
          float c1 = __builtin_amdgcn_exp2f(ml[0] - mn);
          mrun = mn;
          lrun = lrun * c0 + ml[1] * c1;
          if (PBF16) {
            c1 *= ml[1];
#pragma unroll
            for (int e = 0; e < 8; ++e) {
              const unsigned x = pa[(e >> 1) % NPK][2 * (e & 1)], y = pa[(e >> 1) % NPK][2 * (e & 1) + 1];
              const f32x4 v = {__uint_as_float(x << 16), __uint_as_float(x & 0xffff0000u), __uint_as_float(y << 16), __uint_as_float(y & 0xffff0000u)};
              acc[e] = acc[e] * c0 + v * c1;
            }
          } else {
#pragma unroll
            for (int e = 0; e < 8; ++e) acc[e] = acc[e] * c0 + __builtin_bit_cast(f32x4, pa[e % NPK]) * c1;
          }
        }
        if (mine && lane < 32) __hip_atomic_store(ef + 2 * k, e0 + TAG_STEP, RLX_AGENT);
      }
      STAMP(5);
    }
    const float inv = 1.0f / lrun;
    __bf16* orow = out + ((size_t)(b * HQ + h * 4 + w) * SQ + i) * D_ + 4 * g;
#pragma unroll
    for (int e = 0; e < 8; ++e) {
      const f32x4 o = acc[e] * inv;
      u32x2 pk = {cvtpk(o[0], o[1]), cvtpk(o[2], o[3])};
      *(u32x2*)(orow + 16 * e) = pk;
    }
  }
}

extern "C" void kernel_launch(void* const* d_in, const int* in_sizes, int n_in, void* d_out, int out_size, void* d_ws,
                              size_t ws_size, hipStream_t stream) {
  const unsigned kv_bytes = (unsigned)in_sizes[1] * 2u;
  (void)hipFuncSetAttribute((const void*)attn_paged, hipFuncAttributeMaxDynamicSharedMemorySize, LDS_BYTES);
  attn_paged<<<256, 320, LDS_BYTES, stream>>>((const __bf16*)d_in[0], (const __bf16*)d_in[1], (const __bf16*)d_in[2],
                                              (const int*)d_in[3], (const int*)d_in[4], (__bf16*)d_out, (char*)d_ws, kv_bytes);
}
```

```cpp
#include <hip/hip_runtime.h>
#include <hip/hip_bf16.h>
#include <stdint.h>

typedef __bf16 bf16x8 __attribute__((ext_vector_type(8)));
typedef __bf16 bf16x4 __attribute__((ext_vector_type(4)));
typedef __bf16 bf16x2 __attribute__((ext_vector_type(2)));
typedef float f32x4 __attribute__((ext_vector_type(4)));
typedef float f32x2 __attribute__((ext_vector_type(2)));
typedef unsigned u32x4 __attribute__((ext_vector_type(4)));
typedef unsigned u32x2 __attribute__((ext_vector_type(2)));
typedef int i32x2 __attribute__((ext_vector_type(2)));
typedef unsigned long long u64;
#define LAS __attribute__((address_space(3)))
#define GAS __attribute__((address_space(1)))
#define DI __device__ __forceinline__
#define RLX_AGENT __ATOMIC_RELAXED, __HIP_MEMORY_SCOPE_AGENT

constexpr int HQ = 32, SQ = 16, D_ = 128, PAGE = 16, NBPS = 256, SKV = NBPS * PAGE;
constexpr int NSLOT = 8, SLOT_BYTES = 16384;
constexpr int BT_OFF = NSLOT * SLOT_BYTES;
constexpr int CTL_OFF = BT_OFF + 16384;
constexpr int LDS_BYTES = CTL_OFF + 512;
#define WIN 22
#define EXTRA 2
#define PUB_EARLY 2
#define PBF16 1
#define THR 8.0f
constexpr unsigned SPIN_CAP = 1u << 18;
constexpr float SCALE_LOG2E = 0.08838834764831845f * 1.4426950408889634f;
constexpr size_t WS_FLAG = 0, WS_PART = 8 * 4 * 32 * 16;
constexpr int NPK = PBF16 ? 4 : 8;
constexpr unsigned PART_BYTES = NPK * 1024 + 512;
constexpr u64 TAG_STEP = 0x9E3779B97F4A7C15ull;

#define STAMP(k)
#define STAMP_FLUSH(base, cnt)

DI unsigned cvtpk(float lo, float hi) {
  f32x2 v = {lo, hi};
  bf16x2 b = __builtin_convertvector(v, bf16x2);
  return __builtin_bit_cast(unsigned, b);
}
DI float fmax2(float a, float b) { return __builtin_elementwise_maximum(a, b); }
DI float fmax3(float a, float b, float c) { return fmax2(fmax2(a, b), c); }
DI float xg_max(float x) {
  auto s = __builtin_amdgcn_permlane16_swap(__float_as_uint(x), __float_as_uint(x), false, false);
  x = fmax2(__uint_as_float(s[0]), __uint_as_float(s[1]));
  auto t = __builtin_amdgcn_permlane32_swap(__float_as_uint(x), __float_as_uint(x), false, false);
  return fmax2(__uint_as_float(t[0]), __uint_as_float(t[1]));
}
DI int row_scan(int v) {
  v += __builtin_amdgcn_update_dpp(0, v, 0x111, 0xf, 0xf, true);
  v += __builtin_amdgcn_update_dpp(0, v, 0x112, 0xf, 0xf, true);
  v += __builtin_amdgcn_update_dpp(0, v, 0x114, 0xf, 0xf, true);
  v += __builtin_amdgcn_update_dpp(0, v, 0x118, 0xf, 0xf, true);
  return v;
}
DI int rl(int v, int l) { return __builtin_amdgcn_readlane(v, l); }

#define KPOL "nt"
#define VPOL ""
template <int W, bool ISV> DI void dma_piece(unsigned so, unsigned m0v, unsigned voff, __amdgpu_buffer_rsrc_t srd) {
  if constexpr (ISV)
    asm volatile("s_mov_b32 m0, %1\n\ts_waitcnt vmcnt(%4)\n\tbuffer_load_dwordx4 %2, %3, %0 offen " VPOL " lds"
                 :: "s"(so), "s"(m0v), "v"(voff), "s"(srd), "i"(W - 1) : "memory");
  else
    asm volatile("s_mov_b32 m0, %1\n\ts_waitcnt vmcnt(%4)\n\tbuffer_load_dwordx4 %2, %3, %0 offen " KPOL " lds"
                 :: "s"(so), "s"(m0v), "v"(voff), "s"(srd), "i"(W - 1) : "memory");
}
DI void lds_store(unsigned addr, unsigned val) { asm volatile("ds_write_b32 %0, %1" :: "v"(addr), "v"(val) : "memory"); }

__global__ void __launch_bounds__(320, 2)
attn_paged(const __bf16* __restrict__ q, const __bf16* __restrict__ kc, const __bf16* __restrict__ vc,
           const int* __restrict__ bt, const int* __restrict__ lens, __bf16* __restrict__ out, char* ws, unsigned kv_bytes) {
  STAMP(0);
  const int tid = threadIdx.x, lane = tid & 63;
  const int w = __builtin_amdgcn_readfirstlane(tid >> 6);
  const int g = lane >> 4, i = lane & 15;
  const int h = blockIdx.x & 7, j = blockIdx.x >> 3;
  constexpr unsigned ldsbase = 0;

  const int rawlen = lens[i];
  if (w == 4) {
    lds_store(ldsbase + CTL_OFF + 4 * lane, 0u);
    lds_store(ldsbase + CTL_OFF + 256 + 4 * lane, 0u);
  } else {
    const __amdgpu_buffer_rsrc_t srdB = __builtin_amdgcn_make_buffer_rsrc((void*)bt, (short)0, 16 * NBPS * 4, 0x00020000);
    asm volatile("s_mov_b32 m0, %0\n\ts_nop 0\n\t"
                 "buffer_load_dwordx4 %1, %2, %3 offen lds\n\t"
                 "buffer_load_dwordx4 %1, %2, %3 offen offset:1024 lds\n\t"
                 "buffer_load_dwordx4 %1, %2, %3 offen offset:2048 lds\n\t"
                 "buffer_load_dwordx4 %1, %2, %3 offen offset:3072 lds"
                 :: "s"(ldsbase + BT_OFF + w * 4096), "v"(lane * 16), "s"(srdB), "s"(w * 4096) : "memory");
  }
  const int lenv = (rawlen <= 0 || rawlen > SKV) ? SKV : rawlen;
  const int nb = (lenv + 31) >> 5;
  const int nr = max(nb - EXTRA, 1);
  const int Pin = row_scan(nr), P = Pin - nr;
  const int R = rl(Pin, 15);
  const int lo = (j * R) >> 5, hi = ((j + 1) * R) >> 5;
  const int a0 = max(lo, P);
  const int cred = min(hi, Pin) - a0;
  const bool starts = (a0 == P);
  const int sfirst = starts ? 0 : (nb - nr) + a0 - P;
  const int scnt = cred + (starts ? nb - nr : 0);
  const unsigned segmask = (unsigned)__ballot(cred > 0) & 0xffffu;
  asm volatile("s_waitcnt vmcnt(0) lgkmcnt(0)\n\ts_barrier" ::: "memory");

  if (w == 4) {
    __builtin_amdgcn_s_setprio(3);
    int n = 0;
    int off0 = 0, off1 = 0, last0 = 0, last1 = 0;
    for (unsigned m = segmask; m; m &= m - 1) {
      const int b = __builtin_ctz(m);
      const int f = rl(sfirst, b) - n, lp = b * NBPS + ((rl(lenv, b) + 15) >> 4) - 1;
      if (lane >= n) { off0 = b * NBPS + 2 * min(f + lane, 127); last0 = lp; }
      if (lane + 64 >= n) { off1 = b * NBPS + 2 * min(f + lane + 64, 127); last1 = lp; }
      n += rl(scnt, b);
    }
    const i32x2 pg0 = *(const LAS i32x2*)(size_t)(ldsbase + BT_OFF + 4 * off0);
    const i32x2 pg1 = *(const LAS i32x2*)(size_t)(ldsbase + BT_OFF + 4 * off1);
    unsigned oA = (unsigned)pg0[0] << 15, oB = (unsigned)(off0 < last0 ? pg0[1] : pg0[0]) << 15;
    const unsigned oA1 = (unsigned)pg1[0] << 15, oB1 = (unsigned)(off1 < last1 ? pg1[1] : pg1[0]) << 15;
    const __amdgpu_buffer_rsrc_t srdK =
        __builtin_amdgcn_make_buffer_rsrc((void*)((const char*)kc + h * 256), (short)0, kv_bytes - h * 256, 0x00020000);
    const __amdgpu_buffer_rsrc_t srdV =
        __builtin_amdgcn_make_buffer_rsrc((void*)((const char*)vc + h * 256), (short)0, kv_bytes - h * 256, 0x00020000);
    unsigned vK[4], vV[2];
#pragma unroll
    for (int p = 0; p < 4; ++p) vK[p] = (4 * p + g) * 2048 + 16 * (i ^ (4 * p + g));
#pragma unroll
    for (int p = 0; p < 2; ++p) vV[p] = (4 * p + g) * 2048 + 16 * (i ^ ((4 * p + g) << 1));
    const unsigned fulladdr = ldsbase + CTL_OFF + 4 * lane;
    STAMP(1);
    int freed = 0;
    for (int t = 0; t < n; ++t) {
      if (t == 64) { oA = oA1; oB = oB1; }
      unsigned a = rl(oA, t & 63), b = rl(oB, t & 63);
      for (unsigned spins = 0; freed < t - NSLOT + 1 && spins < SPIN_CAP; ++spins) {
        u32x4 d;
        asm volatile("ds_read_b128 %0, %1\n\ts_waitcnt lgkmcnt(0)" : "=v"(d) : "v"(ldsbase + CTL_OFF + 256) : "memory");
        freed = __builtin_amdgcn_readfirstlane(min(min(d[0], d[1]), min(d[2], d[3])));
      }
      asm volatile("s_nop 4" : "+s"(a), "+s"(b));
      const unsigned mb = ldsbase + (t & (NSLOT - 1)) * SLOT_BYTES;
#pragma unroll
      for (int u = 0; u < 16; ++u) {
        const unsigned so = ((u & 4) ? b : a) + ((u & 8) ? (u & 2) * 8192 : 0);
        if (u & 8) dma_piece<WIN, true>(so, mb + u * 1024, vV[u & 1], srdV); else dma_piece<WIN, false>(so, mb + u * 1024, vK[u & 3], srdK);
        if (((u + 1 - WIN) & 7) == 0) lds_store(fulladdr, (unsigned)max((16 * t + u + 1 - WIN) >> 3, 0));
      }
    }
    STAMP(2);
    if (WIN > 24) { asm volatile("s_waitcnt vmcnt(24)" ::: "memory"); lds_store(fulladdr, (unsigned)max(2 * n - 3, 0)); }
    asm volatile("s_waitcnt vmcnt(16)" ::: "memory");
    lds_store(fulladdr, (unsigned)max(2 * n - 2, 0));
    asm volatile("s_waitcnt vmcnt(8)" ::: "memory");
    lds_store(fulladdr, (unsigned)(2 * n - 1));
    asm volatile("s_waitcnt vmcnt(0)" ::: "memory");
    lds_store(fulladdr, (unsigned)(2 * n));
    STAMP(3);
    STAMP_FLUSH(0, 8);
    return;
  }

  if (segmask == 0) return;
  const unsigned rdK = ldsbase + i * 256 + 16 * (g ^ i);
  const int vrow = 4 * g + (i >> 2);
  const unsigned rdV = ldsbase + 8192 + vrow * 256 + ((vrow & 7) << 5) + 8 * (i & 3);
  const unsigned doneaddr = ldsbase + CTL_OFF + 256 + 4 * w;
  volatile LAS unsigned* fullp = (volatile LAS unsigned*)(size_t)(ldsbase + CTL_OFF);
  bf16x8 ones;
#pragma unroll
  for (int k = 0; k < 8; ++k) ones[k] = (__bf16)1.0f;
  const __amdgpu_buffer_rsrc_t srdP = __builtin_amdgcn_make_buffer_rsrc((void*)(ws + WS_PART), (short)0, 8 * 32 * 4 * PART_BYTES, 0x00020000);
  GAS u64* ef = (GAS u64*)(ws + WS_FLAG) + (h * 4 + w) * 64;
  const u64 mytag = __hip_atomic_load(ef + 2 * j, RLX_AGENT) + TAG_STEP;
  const __bf16* qbase = q + ((size_t)(h * 4 + w) * SQ + i) * D_ + 8 * g;
  const int ntot = rl(row_scan(cred > 0 ? scnt : 0), 15);

  bf16x8 qn[4];
#pragma unroll
  for (int s = 0; s < 4; ++s) qn[s] = *(const bf16x8*)(qbase + (size_t)__builtin_ctz(segmask) * HQ * SQ * D_ + 32 * s);

  u32x4 park[NPK];
  f32x2 parkml = {0.f, 0.f};
#pragma unroll
  for (int e = 0; e < NPK; ++e) park[e] = u32x4{0u, 0u, 0u, 0u};
  int pstate = 0;
  const unsigned pbase = ((h * 32 + j) * 4 + w) * PART_BYTES;
  auto publish = [&]() __attribute__((always_inline)) {
#pragma unroll
    for (int e = 0; e < NPK; ++e) __builtin_amdgcn_raw_buffer_store_b128(park[e], srdP, pbase + lane * 16 + e * 1024, 0, 16);
    __builtin_amdgcn_raw_buffer_store_b64(__builtin_bit_cast(u32x2, parkml), srdP, pbase + NPK * 1024 + lane * 8, 0, 16);
    pstate = 2;
  };

  int tg = 0;
  unsigned full = 0;
  for (unsigned m = segmask; m;) {
    const int b = __builtin_ctz(m);
    m &= m - 1;
    const int first = rl(sfirst, b), cnt = rl(scnt, b), len = rl(lenv, b);
    const bool zq = rl(rawlen, b) <= 0;
    bf16x8 qf[4];
#pragma unroll
    for (int s = 0; s < 4; ++s) qf[s] = qn[s];
    asm volatile("" : "+v"(qf[0]), "+v"(qf[1]), "+v"(qf[2]), "+v"(qf[3]));
    {
      const int bn = m ? __builtin_ctz(m) : b;
#pragma unroll
      for (int s = 0; s < 4; ++s) qn[s] = *(const bf16x8*)(qbase + (size_t)bn * HQ * SQ * D_ + 32 * s);
    }
    f32x4 acc[8];
#pragma unroll
    for (int e = 0; e < 8; ++e) acc[e] = f32x4{0.f, 0.f, 0.f, 0.f};
    f32x4 lacc = {0.f, 0.f, 0.f, 0.f};
    float mrun = -1e30f;

    for (int t = 0; t < cnt; ++t, ++tg) {
      if (__builtin_expect(pstate == 1 && tg + PUB_EARLY >= ntot, 0)) publish();
      for (unsigned spins = 0; full < (unsigned)(2 * tg + 1) && spins < SPIN_CAP; ++spins) full = __builtin_amdgcn_readfirstlane(*fullp);
      asm volatile("" ::: "memory");
      if (tg == 0) STAMP(1);
      const unsigned so = (tg & (NSLOT - 1)) * SLOT_BYTES;
      const unsigned ak = rdK + so, av = rdV + so;
      bf16x8 kA[4], kB[4];
#pragma unroll
      for (int s = 0; s < 4; ++s) {
        kA[s] = *(const LAS bf16x8*)(size_t)(ak ^ (s << 6));
        kB[s] = *(const LAS bf16x8*)(size_t)((ak ^ (s << 6)) + 4096);
      }
      f32x4 sA = {0.f, 0.f, 0.f, 0.f}, sB = {0.f, 0.f, 0.f, 0.f};
#pragma unroll
      for (int s = 0; s < 4; ++s) {
        sA = __builtin_amdgcn_mfma_f32_16x16x32_bf16(kA[s], qf[s], sA, 0, 0, 0);
        sB = __builtin_amdgcn_mfma_f32_16x16x32_bf16(kB[s], qf[s], sB, 0, 0, 0);
      }
      const int kbase = (first + t) * 32;
      if (__builtin_expect(kbase + 32 > len || zq, 0)) {
        const int lim = len - kbase - 4 * g;
#pragma unroll
        for (int r = 0; r < 4; ++r) {
          sA[r] = zq ? 0.f : ((r < lim) ? sA[r] : -INFINITY);
          sB[r] = zq ? 0.f : ((r + 16 < lim) ? sB[r] : -INFINITY);
        }
      }
      float mx = fmax3(fmax3(sA[0], sA[1], sA[2]), fmax3(sA[3], sB[0], sB[1]), fmax2(sB[2], sB[3]));
      mx *= SCALE_LOG2E;
      if (__builtin_expect(__any(mx > mrun + THR), 0)) {
        mx = xg_max(mx);
        const float mn = fmax2(mrun, mx);
        const float alpha = __builtin_amdgcn_exp2f(mrun - mn);
        mrun = mn;
#pragma unroll
        for (int e = 0; e < 8; ++e) acc[e] *= alpha;
        lacc *= alpha;
      }
      float p[8];
#pragma unroll
      for (int r = 0; r < 4; ++r) {
        p[r] = __builtin_amdgcn_exp2f(fmaf(sA[r], SCALE_LOG2E, -mrun));
        p[4 + r] = __builtin_amdgcn_exp2f(fmaf(sB[r], SCALE_LOG2E, -mrun));
      }
      u32x4 pw = {cvtpk(p[0], p[1]), cvtpk(p[2], p[3]), cvtpk(p[4], p[5]), cvtpk(p[6], p[7])};
      asm volatile("" : "+v"(pw));
      const bf16x8 pf = __builtin_bit_cast(bf16x8, pw);
      for (unsigned spins = 0; full < (unsigned)(2 * tg + 2) && spins < SPIN_CAP; ++spins) full = __builtin_amdgcn_readfirstlane(*fullp);
      asm volatile("" ::: "memory");
      bf16x4 vA[8], vB[8];
#pragma unroll
      for (int e = 0; e < 8; ++e) {
        vA[e] = __builtin_amdgcn_ds_read_tr16_b64_v4bf16((LAS bf16x4*)(size_t)(av ^ (e << 5)));
        vB[e] = __builtin_amdgcn_ds_read_tr16_b64_v4bf16((LAS bf16x4*)(size_t)((av ^ (e << 5)) + 4096));
      }
      lds_store(doneaddr, (unsigned)(tg + 1));
      lacc = __builtin_amdgcn_mfma_f32_16x16x32_bf16(ones, pf, lacc, 0, 0, 0);
#pragma unroll
      for (int e = 0; e < 8; ++e) {
        const bf16x8 vf = __builtin_shufflevector(vA[e], vB[e], 0, 1, 2, 3, 4, 5, 6, 7);
        acc[e] = __builtin_amdgcn_mfma_f32_16x16x32_bf16(vf, pf, acc[e], 0, 0, 0);
      }
    }

    float lrun = lacc[0];
    if (first != 0) {
      if (PBF16) {
        const float inv = __builtin_amdgcn_rcpf(lrun);
#pragma unroll
        for (int e = 0; e < 4; ++e) {
          const f32x4 x = acc[2 * e] * inv, y = acc[2 * e + 1] * inv;
          park[e] = u32x4{cvtpk(x[0], x[1]), cvtpk(x[2], x[3]), cvtpk(y[0], y[1]), cvtpk(y[2], y[3])};
        }
      } else {
#pragma unroll
        for (int e = 0; e < NPK; ++e) park[e] = __builtin_bit_cast(u32x4, acc[e & 7]);
      }
      parkml = f32x2{mrun, lrun};
      pstate = 1;
      if (m) continue;
    }
    if (!m) {
      STAMP(2);
      if (pstate == 1) publish();
      if (pstate == 2) {
        asm volatile("s_waitcnt vmcnt(0)" ::: "memory");
        if (lane == 0) __hip_atomic_store(ef + 2 * j + 1, mytag, RLX_AGENT);
      }
      STAMP(3);
      if (first != 0) break;
      const int Pe = rl(Pin, b);
      const int k = lane & 31;
      const bool mine = k > j && ((k * R) >> 5) < Pe && (((k + 1) * R) >> 5) > ((k * R) >> 5);
      const unsigned hmask = (unsigned)__ballot(mine);
      if (hmask) {
        u64 e0 = 0;
        for (unsigned spins = 0; spins < SPIN_CAP; ++spins) {
          e0 = __hip_atomic_load(ef + 2 * k, RLX_AGENT);
          const u64 f0 = __hip_atomic_load(ef + 2 * k + 1, RLX_AGENT);
          if (__all(!mine || f0 == e0 + TAG_STEP)) break;
          __builtin_amdgcn_s_sleep(1);
        }
        asm volatile("" ::: "memory");
        STAMP(4);
#pragma unroll 1
        for (unsigned hm = hmask; hm; hm &= hm - 1) {
          const unsigned base = ((h * 32 + __builtin_ctz(hm)) * 4 + w) * PART_BYTES;
          u32x4 pa[NPK];
          const f32x2 ml = __builtin_bit_cast(f32x2, __builtin_amdgcn_raw_buffer_load_b64(srdP, base + NPK * 1024 + lane * 8, 0, 16));
#pragma unroll
          for (int e = 0; e < NPK; ++e) pa[e] = __builtin_amdgcn_raw_buffer_load_b128(srdP, base + lane * 16 + e * 1024, 0, 16);
          const float mn = fmax2(mrun, ml[0]);
          const float c0 = __builtin_amdgcn_exp2f(mrun - mn);
          float c1 = __builtin_amdgcn_exp2f(ml[0] - mn);
          mrun = mn;
          lrun = lrun * c0 + ml[1] * c1;
          if (PBF16) {
            c1 *= ml[1];
#pragma unroll
            for (int e = 0; e < 8; ++e) {
              const unsigned x = pa[(e >> 1) % NPK][2 * (e & 1)], y = pa[(e >> 1) % NPK][2 * (e & 1) + 1];
              const f32x4 v = {__uint_as_float(x << 16), __uint_as_float(x & 0xffff0000u), __uint_as_float(y << 16), __uint_as_float(y & 0xffff0000u)};
              acc[e] = acc[e] * c0 + v * c1;
            }
          } else {
#pragma unroll
            for (int e = 0; e < 8; ++e) acc[e] = acc[e] * c0 + __builtin_bit_cast(f32x4, pa[e % NPK]) * c1;
          }
        }
        if (mine && lane < 32) __hip_atomic_store(ef + 2 * k, e0 + TAG_STEP, RLX_AGENT);
      }
      STAMP(5);
    }
    const float inv = 1.0f / lrun;
    __bf16* orow = out + ((size_t)(b * HQ + h * 4 + w) * SQ + i) * D_ + 4 * g;
#pragma unroll
    for (int e = 0; e < 8; ++e) {
      const f32x4 o = acc[e] * inv;
      u32x2 pk = {cvtpk(o[0], o[1]), cvtpk(o[2], o[3])};
      *(u32x2*)(orow + 16 * e) = pk;
    }
  }
}

extern "C" void kernel_launch(void* const* d_in, const int* in_sizes, int n_in, void* d_out, int out_size, void* d_ws,
                              size_t ws_size, hipStream_t stream) {
  const unsigned kv_bytes = (unsigned)in_sizes[1] * 2u;
  (void)hipFuncSetAttribute((const void*)attn_paged, hipFuncAttributeMaxDynamicSharedMemorySize, LDS_BYTES);
  attn_paged<<<256, 320, LDS_BYTES, stream>>>((const __bf16*)d_in[0], (const __bf16*)d_in[1], (const __bf16*)d_in[2],
                                              (const int*)d_in[3], (const int*)d_in[4], (__bf16*)d_out, (char*)d_ws, kv_bytes);
}
```

```cpp
#define PROBE 0
#define PROBE_L 1
#define PROBE_MUL 0
#include <hip/hip_runtime.h>
#include <hip/hip_bf16.h>
#include <stdint.h>

typedef __bf16 bf16x8 __attribute__((ext_vector_type(8)));
typedef __bf16 bf16x4 __attribute__((ext_vector_type(4)));
typedef __bf16 bf16x2 __attribute__((ext_vector_type(2)));
typedef float f32x4 __attribute__((ext_vector_type(4)));
typedef float f32x2 __attribute__((ext_vector_type(2)));
typedef unsigned u32x4 __attribute__((ext_vector_type(4)));
typedef unsigned u32x2 __attribute__((ext_vector_type(2)));
typedef int i32x2 __attribute__((ext_vector_type(2)));
typedef unsigned long long u64;
#define LAS __attribute__((address_space(3)))
#define GAS __attribute__((address_space(1)))
#define DI __device__ __forceinline__
#define RLX_AGENT __ATOMIC_RELAXED, __HIP_MEMORY_SCOPE_AGENT

constexpr int HQ = 32, SQ = 16, D_ = 128, PAGE = 16, NBPS = 256, SKV = NBPS * PAGE;
constexpr int NSLOT = 8, SLOT_BYTES = 16384;
constexpr int BT_OFF = NSLOT * SLOT_BYTES;
constexpr int CTL_OFF = BT_OFF + 16384;
constexpr int LDS_BYTES = CTL_OFF + 512;
#define WIN 22
#define EXTRA 2
#define PUB_EARLY 2
#define PBF16 1
#define THR 8.0f
constexpr unsigned SPIN_CAP = 1u << 18;
constexpr float SCALE_LOG2E = 0.08838834764831845f * 1.4426950408889634f;
constexpr size_t WS_FLAG = 0, WS_PART = 8 * 4 * 32 * 16;
constexpr int NPK = PBF16 ? 4 : 8;
constexpr unsigned PART_BYTES = NPK * 1024 + 512;
constexpr u64 TAG_STEP = 0x9E3779B97F4A7C15ull;

#define STAMP(k) do { const u64 t_ = __builtin_amdgcn_s_memrealtime(); __builtin_amdgcn_s_waitcnt(0xC07F); stv[k] = t_; } while (0)
#define STAMP_FLUSH(base, cnt)
#define PROBE_END(isload) do { if ((isload) == PROBE_L) { const u64 q_ = stv[PROBE] ? stv[PROBE] - stv[0] : 0; const u64 until_ = stv[0] + 6000 + PROBE_MUL * q_; while (__builtin_amdgcn_s_memrealtime() < until_) __builtin_amdgcn_s_sleep(1); } } while (0)

DI unsigned cvtpk(float lo, float hi) {
  f32x2 v = {lo, hi};
  bf16x2 b = __builtin_convertvector(v, bf16x2);
  return __builtin_bit_cast(unsigned, b);
}
DI float fmax2(float a, float b) { return __builtin_elementwise_maximum(a, b); }
DI float fmax3(float a, float b, float c) { return fmax2(fmax2(a, b), c); }
DI float xg_max(float x) {
  auto s = __builtin_amdgcn_permlane16_swap(__float_as_uint(x), __float_as_uint(x), false, false);
  x = fmax2(__uint_as_float(s[0]), __uint_as_float(s[1]));
  auto t = __builtin_amdgcn_permlane32_swap(__float_as_uint(x), __float_as_uint(x), false, false);
  return fmax2(__uint_as_float(t[0]), __uint_as_float(t[1]));
}
DI int row_scan(int v) {
  v += __builtin_amdgcn_update_dpp(0, v, 0x111, 0xf, 0xf, true);
  v += __builtin_amdgcn_update_dpp(0, v, 0x112, 0xf, 0xf, true);
  v += __builtin_amdgcn_update_dpp(0, v, 0x114, 0xf, 0xf, true);
  v += __builtin_amdgcn_update_dpp(0, v, 0x118, 0xf, 0xf, true);
  return v;
}
DI int rl(int v, int l) { return __builtin_amdgcn_readlane(v, l); }

#define KPOL "nt"
#define VPOL "nt"
template <int W, bool ISV> DI void dma_piece(unsigned so, unsigned m0v, unsigned voff, __amdgpu_buffer_rsrc_t srd) {
  if constexpr (ISV)
    asm volatile("s_mov_b32 m0, %1\n\ts_waitcnt vmcnt(%4)\n\tbuffer_load_dwordx4 %2, %3, %0 offen " VPOL " lds"
                 :: "s"(so), "s"(m0v), "v"(voff), "s"(srd), "i"(W - 1) : "memory");
  else
    asm volatile("s_mov_b32 m0, %1\n\ts_waitcnt vmcnt(%4)\n\tbuffer_load_dwordx4 %2, %3, %0 offen " KPOL " lds"
                 :: "s"(so), "s"(m0v), "v"(voff), "s"(srd), "i"(W - 1) : "memory");
}
DI void lds_store(unsigned addr, unsigned val) { asm volatile("ds_write_b32 %0, %1" :: "v"(addr), "v"(val) : "memory"); }

__global__ void __launch_bounds__(320, 2)
attn_paged(const __bf16* __restrict__ q, const __bf16* __restrict__ kc, const __bf16* __restrict__ vc,
           const int* __restrict__ bt, const int* __restrict__ lens, __bf16* __restrict__ out, char* ws, unsigned kv_bytes) {
  u64 stv[8] = {0, 0, 0, 0, 0, 0, 0, 0};
  STAMP(0);
  const int tid = threadIdx.x, lane = tid & 63;
  const int w = __builtin_amdgcn_readfirstlane(tid >> 6);
  const int g = lane >> 4, i = lane & 15;
  const int h = blockIdx.x & 7, j = blockIdx.x >> 3;
  constexpr unsigned ldsbase = 0;

  const int rawlen = lens[i];
  if (w == 4) {
    lds_store(ldsbase + CTL_OFF + 4 * lane, 0u);
    lds_store(ldsbase + CTL_OFF + 256 + 4 * lane, 0u);
  } else {
    const __amdgpu_buffer_rsrc_t srdB = __builtin_amdgcn_make_buffer_rsrc((void*)bt, (short)0, 16 * NBPS * 4, 0x00020000);
    asm volatile("s_mov_b32 m0, %0\n\ts_nop 0\n\t"
                 "buffer_load_dwordx4 %1, %2, %3 offen lds\n\t"
                 "buffer_load_dwordx4 %1, %2, %3 offen offset:1024 lds\n\t"
                 "buffer_load_dwordx4 %1, %2, %3 offen offset:2048 lds\n\t"
                 "buffer_load_dwordx4 %1, %2, %3 offen offset:3072 lds"
                 :: "s"(ldsbase + BT_OFF + w * 4096), "v"(lane * 16), "s"(srdB), "s"(w * 4096) : "memory");
  }
  const int lenv = (rawlen <= 0 || rawlen > SKV) ? SKV : rawlen;
  const int nb = (lenv + 31) >> 5;
  const int nr = max(nb - EXTRA, 1);
  const int Pin = row_scan(nr), P = Pin - nr;
  const int R = rl(Pin, 15);
  const int lo = (j * R) >> 5, hi = ((j + 1) * R) >> 5;
  const int a0 = max(lo, P);
  const int cred = min(hi, Pin) - a0;
  const bool starts = (a0 == P);
  const int sfirst = starts ? 0 : (nb - nr) + a0 - P;
  const int scnt = cred + (starts ? nb - nr : 0);
  const unsigned segmask = (unsigned)__ballot(cred > 0) & 0xffffu;
  asm volatile("s_waitcnt vmcnt(0) lgkmcnt(0)\n\ts_barrier" ::: "memory");

  if (w == 4) {
    __builtin_amdgcn_s_setprio(3);
    int n = 0;
    int off0 = 0, off1 = 0, last0 = 0, last1 = 0;
    for (unsigned m = segmask; m; m &= m - 1) {
      const int b = __builtin_ctz(m);
      const int f = rl(sfirst, b) - n, lp = b * NBPS + ((rl(lenv, b) + 15) >> 4) - 1;
      if (lane >= n) { off0 = b * NBPS + 2 * min(f + lane, 127); last0 = lp; }
      if (lane + 64 >= n) { off1 = b * NBPS + 2 * min(f + lane + 64, 127); last1 = lp; }
      n += rl(scnt, b);
    }
    const i32x2 pg0 = *(const LAS i32x2*)(size_t)(ldsbase + BT_OFF + 4 * off0);
    const i32x2 pg1 = *(const LAS i32x2*)(size_t)(ldsbase + BT_OFF + 4 * off1);
    unsigned oA = (unsigned)pg0[0] << 15, oB = (unsigned)(off0 < last0 ? pg0[1] : pg0[0]) << 15;
    const unsigned oA1 = (unsigned)pg1[0] << 15, oB1 = (unsigned)(off1 < last1 ? pg1[1] : pg1[0]) << 15;
    const __amdgpu_buffer_rsrc_t srdK =
        __builtin_amdgcn_make_buffer_rsrc((void*)((const char*)kc + h * 256), (short)0, kv_bytes - h * 256, 0x00020000);
    const __amdgpu_buffer_rsrc_t srdV =
        __builtin_amdgcn_make_buffer_rsrc((void*)((const char*)vc + h * 256), (short)0, kv_bytes - h * 256, 0x00020000);
    unsigned vK[4], vV[2];
#pragma unroll
    for (int p = 0; p < 4; ++p) vK[p] = (4 * p + g) * 2048 + 16 * (i ^ (4 * p + g));
#pragma unroll
    for (int p = 0; p < 2; ++p) vV[p] = (4 * p + g) * 2048 + 16 * (i ^ ((4 * p + g) << 1));
    const unsigned fulladdr = ldsbase + CTL_OFF + 4 * lane;
    STAMP(1);
    int freed = 0;
    for (int t = 0; t < n; ++t) {
      if (t == 64) { oA = oA1; oB = oB1; }
      unsigned a = rl(oA, t & 63), b = rl(oB, t & 63);
      for (unsigned spins = 0; freed < t - NSLOT + 1 && spins < SPIN_CAP; ++spins) {
        u32x4 d;
        asm volatile("ds_read_b128 %0, %1\n\ts_waitcnt lgkmcnt(0)" : "=v"(d) : "v"(ldsbase + CTL_OFF + 256) : "memory");
        freed = __builtin_amdgcn_readfirstlane(min(min(d[0], d[1]), min(d[2], d[3])));
      }
      asm volatile("s_nop 4" : "+s"(a), "+s"(b));
      const unsigned mb = ldsbase + (t & (NSLOT - 1)) * SLOT_BYTES;
#pragma unroll
      for (int u = 0; u < 16; ++u) {
        const unsigned so = ((u & 4) ? b : a) + ((u & 8) ? (u & 2) * 8192 : 0);
        if (u & 8) dma_piece<WIN, true>(so, mb + u * 1024, vV[u & 1], srdV); else dma_piece<WIN, false>(so, mb + u * 1024, vK[u & 3], srdK);
        if (((u + 1 - WIN) & 7) == 0) lds_store(fulladdr, (unsigned)max((16 * t + u + 1 - WIN) >> 3, 0));
      }
    }
    STAMP(2);
    if (WIN > 24) { asm volatile("s_waitcnt vmcnt(24)" ::: "memory"); lds_store(fulladdr, (unsigned)max(2 * n - 3, 0)); }
    asm volatile("s_waitcnt vmcnt(16)" ::: "memory");
    lds_store(fulladdr, (unsigned)max(2 * n - 2, 0));
    asm volatile("s_waitcnt vmcnt(8)" ::: "memory");
    lds_store(fulladdr, (unsigned)(2 * n - 1));
    asm volatile("s_waitcnt vmcnt(0)" ::: "memory");
    lds_store(fulladdr, (unsigned)(2 * n));
    STAMP(3);
    STAMP_FLUSH(0, 8);
    PROBE_END(1);
    return;
  }

  if (segmask == 0) return;
  const unsigned rdK = ldsbase + i * 256 + 16 * (g ^ i);
  const int vrow = 4 * g + (i >> 2);
  const unsigned rdV = ldsbase + 8192 + vrow * 256 + ((vrow & 7) << 5) + 8 * (i & 3);
  const unsigned doneaddr = ldsbase + CTL_OFF + 256 + 4 * w;
  volatile LAS unsigned* fullp = (volatile LAS unsigned*)(size_t)(ldsbase + CTL_OFF);
  bf16x8 ones;
#pragma unroll
  for (int k = 0; k < 8; ++k) ones[k] = (__bf16)1.0f;
  const __amdgpu_buffer_rsrc_t srdP = __builtin_amdgcn_make_buffer_rsrc((void*)(ws + WS_PART), (short)0, 8 * 32 * 4 * PART_BYTES, 0x00020000);
  GAS u64* ef = (GAS u64*)(ws + WS_FLAG) + (h * 4 + w) * 64;
  const u64 mytag = __hip_atomic_load(ef + 2 * j, RLX_AGENT) + TAG_STEP;
  const __bf16* qbase = q + ((size_t)(h * 4 + w) * SQ + i) * D_ + 8 * g;
  const int ntot = rl(row_scan(cred > 0 ? scnt : 0), 15);

  bf16x8 qn[4];
#pragma unroll
  for (int s = 0; s < 4; ++s) qn[s] = *(const bf16x8*)(qbase + (size_t)__builtin_ctz(segmask) * HQ * SQ * D_ + 32 * s);

  u32x4 park[NPK];
  f32x2 parkml = {0.f, 0.f};
#pragma unroll
  for (int e = 0; e < NPK; ++e) park[e] = u32x4{0u, 0u, 0u, 0u};
  int pstate = 0;
  const unsigned pbase = ((h * 32 + j) * 4 + w) * PART_BYTES;
  auto publish = [&]() __attribute__((always_inline)) {
#pragma unroll
    for (int e = 0; e < NPK; ++e) __builtin_amdgcn_raw_buffer_store_b128(park[e], srdP, pbase + lane * 16 + e * 1024, 0, 16);
    __builtin_amdgcn_raw_buffer_store_b64(__builtin_bit_cast(u32x2, parkml), srdP, pbase + NPK * 1024 + lane * 8, 0, 16);
    pstate = 2;
  };

  int tg = 0;
  unsigned full = 0;
  for (unsigned m = segmask; m;) {
    const int b = __builtin_ctz(m);
    m &= m - 1;
    const int first = rl(sfirst, b), cnt = rl(scnt, b), len = rl(lenv, b);
    const bool zq = rl(rawlen, b) <= 0;
    bf16x8 qf[4];
#pragma unroll
    for (int s = 0; s < 4; ++s) qf[s] = qn[s];
    asm volatile("" : "+v"(qf[0]), "+v"(qf[1]), "+v"(qf[2]), "+v"(qf[3]));
    {
      const int bn = m ? __builtin_ctz(m) : b;
#pragma unroll
      for (int s = 0; s < 4; ++s) qn[s] = *(const bf16x8*)(qbase + (size_t)bn * HQ * SQ * D_ + 32 * s);
    }
    f32x4 acc[8];
#pragma unroll
    for (int e = 0; e < 8; ++e) acc[e] = f32x4{0.f, 0.f, 0.f, 0.f};
    f32x4 lacc = {0.f, 0.f, 0.f, 0.f};
    float mrun = -1e30f;

    for (int t = 0; t < cnt; ++t, ++tg) {
      if (__builtin_expect(pstate == 1 && tg + PUB_EARLY >= ntot, 0)) publish();
      for (unsigned spins = 0; full < (unsigned)(2 * tg + 1) && spins < SPIN_CAP; ++spins) full = __builtin_amdgcn_readfirstlane(*fullp);
      asm volatile("" ::: "memory");
      if (tg == 0) STAMP(1);
      const unsigned so = (tg & (NSLOT - 1)) * SLOT_BYTES;
      const unsigned ak = rdK + so, av = rdV + so;
      bf16x8 kA[4], kB[4];
#pragma unroll
      for (int s = 0; s < 4; ++s) {
        kA[s] = *(const LAS bf16x8*)(size_t)(ak ^ (s << 6));
        kB[s] = *(const LAS bf16x8*)(size_t)((ak ^ (s << 6)) + 4096);
      }
      f32x4 sA = {0.f, 0.f, 0.f, 0.f}, sB = {0.f, 0.f, 0.f, 0.f};
#pragma unroll
      for (int s = 0; s < 4; ++s) {
        sA = __builtin_amdgcn_mfma_f32_16x16x32_bf16(kA[s], qf[s], sA, 0, 0, 0);
        sB = __builtin_amdgcn_mfma_f32_16x16x32_bf16(kB[s], qf[s], sB, 0, 0, 0);
      }
      const int kbase = (first + t) * 32;
      if (__builtin_expect(kbase + 32 > len || zq, 0)) {
        const int lim = len - kbase - 4 * g;
#pragma unroll
        for (int r = 0; r < 4; ++r) {
          sA[r] = zq ? 0.f : ((r < lim) ? sA[r] : -INFINITY);
          sB[r] = zq ? 0.f : ((r + 16 < lim) ? sB[r] : -INFINITY);
        }
      }
      float mx = fmax3(fmax3(sA[0], sA[1], sA[2]), fmax3(sA[3], sB[0], sB[1]), fmax2(sB[2], sB[3]));
      mx *= SCALE_LOG2E;
      if (__builtin_expect(__any(mx > mrun + THR), 0)) {
        mx = xg_max(mx);
        const float mn = fmax2(mrun, mx);
        const float alpha = __builtin_amdgcn_exp2f(mrun - mn);
        mrun = mn;
#pragma unroll
        for (int e = 0; e < 8; ++e) acc[e] *= alpha;
        lacc *= alpha;
      }
      float p[8];
#pragma unroll
      for (int r = 0; r < 4; ++r) {
        p[r] = __builtin_amdgcn_exp2f(fmaf(sA[r], SCALE_LOG2E, -mrun));
        p[4 + r] = __builtin_amdgcn_exp2f(fmaf(sB[r], SCALE_LOG2E, -mrun));
      }
      u32x4 pw = {cvtpk(p[0], p[1]), cvtpk(p[2], p[3]), cvtpk(p[4], p[5]), cvtpk(p[6], p[7])};
      asm volatile("" : "+v"(pw));
      const bf16x8 pf = __builtin_bit_cast(bf16x8, pw);
      for (unsigned spins = 0; full < (unsigned)(2 * tg + 2) && spins < SPIN_CAP; ++spins) full = __builtin_amdgcn_readfirstlane(*fullp);
      asm volatile("" ::: "memory");
      bf16x4 vA[8], vB[8];
#pragma unroll
      for (int e = 0; e < 8; ++e) {
        vA[e] = __builtin_amdgcn_ds_read_tr16_b64_v4bf16((LAS bf16x4*)(size_t)(av ^ (e << 5)));
        vB[e] = __builtin_amdgcn_ds_read_tr16_b64_v4bf16((LAS bf16x4*)(size_t)((av ^ (e << 5)) + 4096));
      }
      lds_store(doneaddr, (unsigned)(tg + 1));
      lacc = __builtin_amdgcn_mfma_f32_16x16x32_bf16(ones, pf, lacc, 0, 0, 0);
#pragma unroll
      for (int e = 0; e < 8; ++e) {
        const bf16x8 vf = __builtin_shufflevector(vA[e], vB[e], 0, 1, 2, 3, 4, 5, 6, 7);
        acc[e] = __builtin_amdgcn_mfma_f32_16x16x32_bf16(vf, pf, acc[e], 0, 0, 0);
      }
    }

    float lrun = lacc[0];
    if (first != 0) {
      if (PBF16) {
        const float inv = __builtin_amdgcn_rcpf(lrun);
#pragma unroll
        for (int e = 0; e < 4; ++e) {
          const f32x4 x = acc[2 * e] * inv, y = acc[2 * e + 1] * inv;
          park[e] = u32x4{cvtpk(x[0], x[1]), cvtpk(x[2], x[3]), cvtpk(y[0], y[1]), cvtpk(y[2], y[3])};
        }
      } else {
#pragma unroll
        for (int e = 0; e < NPK; ++e) park[e] = __builtin_bit_cast(u32x4, acc[e & 7]);
      }
      parkml = f32x2{mrun, lrun};
      pstate = 1;
      if (m) continue;
    }
    if (!m) {
      STAMP(2);
      if (pstate == 1) publish();
      if (pstate == 2) {
        asm volatile("s_waitcnt vmcnt(0)" ::: "memory");
        if (lane == 0) __hip_atomic_store(ef + 2 * j + 1, mytag, RLX_AGENT);
      }
      STAMP(3);
      if (first != 0) break;
      const int Pe = rl(Pin, b);
      const int k = lane & 31;
      const bool mine = k > j && ((k * R) >> 5) < Pe && (((k + 1) * R) >> 5) > ((k * R) >> 5);
      const unsigned hmask = (unsigned)__ballot(mine);
      if (hmask) {
        u64 e0 = 0;
        for (unsigned spins = 0; spins < SPIN_CAP; ++spins) {
          e0 = __hip_atomic_load(ef + 2 * k, RLX_AGENT);
          const u64 f0 = __hip_atomic_load(ef + 2 * k + 1, RLX_AGENT);
          if (__all(!mine || f0 == e0 + TAG_STEP)) break;
          __builtin_amdgcn_s_sleep(1);
        }
        asm volatile("" ::: "memory");
        STAMP(4);
#pragma unroll 1
        for (unsigned hm = hmask; hm; hm &= hm - 1) {
          const unsigned base = ((h * 32 + __builtin_ctz(hm)) * 4 + w) * PART_BYTES;
          u32x4 pa[NPK];
          const f32x2 ml = __builtin_bit_cast(f32x2, __builtin_amdgcn_raw_buffer_load_b64(srdP, base + NPK * 1024 + lane * 8, 0, 16));
#pragma unroll
          for (int e = 0; e < NPK; ++e) pa[e] = __builtin_amdgcn_raw_buffer_load_b128(srdP, base + lane * 16 + e * 1024, 0, 16);
          const float mn = fmax2(mrun, ml[0]);
          const float c0 = __builtin_amdgcn_exp2f(mrun - mn);
          float c1 = __builtin_amdgcn_exp2f(ml[0] - mn);
          mrun = mn;
          lrun = lrun * c0 + ml[1] * c1;
          if (PBF16) {
            c1 *= ml[1];
#pragma unroll
            for (int e = 0; e < 8; ++e) {
              const unsigned x = pa[(e >> 1) % NPK][2 * (e & 1)], y = pa[(e >> 1) % NPK][2 * (e & 1) + 1];
              const f32x4 v = {__uint_as_float(x << 16), __uint_as_float(x & 0xffff0000u), __uint_as_float(y << 16), __uint_as_float(y & 0xffff0000u)};
              acc[e] = acc[e] * c0 + v * c1;
            }
          } else {
#pragma unroll
            for (int e = 0; e < 8; ++e) acc[e] = acc[e] * c0 + __builtin_bit_cast(f32x4, pa[e % NPK]) * c1;
          }
        }
        if (mine && lane < 32) __hip_atomic_store(ef + 2 * k, e0 + TAG_STEP, RLX_AGENT);
      }
      STAMP(5);
    }
    const float inv = 1.0f / lrun;
    __bf16* orow = out + ((size_t)(b * HQ + h * 4 + w) * SQ + i) * D_ + 4 * g;
#pragma unroll
    for (int e = 0; e < 8; ++e) {
      const f32x4 o = acc[e] * inv;
      u32x2 pk = {cvtpk(o[0], o[1]), cvtpk(o[2], o[3])};
      *(u32x2*)(orow + 16 * e) = pk;
    }
  }
  asm volatile("s_waitcnt vmcnt(0)" ::: "memory");
  STAMP(6);
  PROBE_END(0);
}

extern "C" void kernel_launch(void* const* d_in, const int* in_sizes, int n_in, void* d_out, int out_size, void* d_ws,
                              size_t ws_size, hipStream_t stream) {
  const unsigned kv_bytes = (unsigned)in_sizes[1] * 2u;
  (void)hipFuncSetAttribute((const void*)attn_paged, hipFuncAttributeMaxDynamicSharedMemorySize, LDS_BYTES);
  attn_paged<<<256, 320, LDS_BYTES, stream>>>((const __bf16*)d_in[0], (const __bf16*)d_in[1], (const __bf16*)d_in[2],
                                              (const int*)d_in[3], (const int*)d_in[4], (__bf16*)d_out, (char*)d_ws, kv_bytes);
}
```

```cpp
#include <hip/hip_runtime.h>
#include <hip/hip_bf16.h>
#include <stdint.h>

typedef __bf16 bf16x8 __attribute__((ext_vector_type(8)));
typedef __bf16 bf16x4 __attribute__((ext_vector_type(4)));
typedef __bf16 bf16x2 __attribute__((ext_vector_type(2)));
typedef float f32x4 __attribute__((ext_vector_type(4)));
typedef float f32x2 __attribute__((ext_vector_type(2)));
typedef unsigned u32x4 __attribute__((ext_vector_type(4)));
typedef unsigned u32x2 __attribute__((ext_vector_type(2)));
typedef int i32x2 __attribute__((ext_vector_type(2)));
typedef unsigned long long u64;
typedef float f32x32 __attribute__((ext_vector_type(32)));
typedef unsigned u32x16 __attribute__((ext_vector_type(16)));
typedef unsigned u32x32 __attribute__((ext_vector_type(32)));
#define LAS __attribute__((address_space(3)))
#define GAS __attribute__((address_space(1)))
#define DI __device__ __forceinline__
#define RLX_AGENT __ATOMIC_RELAXED, __HIP_MEMORY_SCOPE_AGENT

constexpr int HQ = 32, SQ = 16, D_ = 128, PAGE = 16, NBPS = 256, SKV = NBPS * PAGE;
constexpr int NSLOT = 8, SLOT_BYTES = 16384;
constexpr int BT_OFF = NSLOT * SLOT_BYTES;
constexpr int CTL_OFF = BT_OFF + 3072;
constexpr int LDS_BYTES = CTL_OFF + 512;
#define WIN 22
#define EXTRA 3
#define PUB_EARLY 5

#define THR 8.0f
#define OUT_AUX 0
#define TLB_TOUCH 0
constexpr unsigned SPIN_CAP = 1u << 18;
constexpr float SCALE_LOG2E = 0.08838834764831845f * 1.4426950408889634f;
constexpr size_t WS_EPOCH = 0, WS_PART = 8192;
constexpr unsigned PART_BYTES = 4096 + 256, PART_TOTAL = 8 * 32 * 4 * PART_BYTES, ML_OFF = 4096, FLAG_OFF = 4096 + 128;
constexpr u64 TAG_STEP = 0x9E3779B97F4A7C15ull;
#define POLL_EARLY 3
#define CODE_BYTES 10176

#define STAMP(k)
#define STAMP_FLUSH(base, cnt)

DI unsigned cvtpk(float lo, float hi) {
  f32x2 v = {lo, hi};
  bf16x2 b = __builtin_convertvector(v, bf16x2);
  return __builtin_bit_cast(unsigned, b);
}
DI float fmax2(float a, float b) { return __builtin_elementwise_maximum(a, b); }
DI float fmax3(float a, float b, float c) { return fmax2(fmax2(a, b), c); }
DI float xg_max(float x) {
  auto s = __builtin_amdgcn_permlane16_swap(__float_as_uint(x), __float_as_uint(x), false, false);
  x = fmax2(__uint_as_float(s[0]), __uint_as_float(s[1]));
  auto t = __builtin_amdgcn_permlane32_swap(__float_as_uint(x), __float_as_uint(x), false, false);
  return fmax2(__uint_as_float(t[0]), __uint_as_float(t[1]));
}
DI int row_scan(int v) {
  v += __builtin_amdgcn_update_dpp(0, v, 0x111, 0xf, 0xf, true);
  v += __builtin_amdgcn_update_dpp(0, v, 0x112, 0xf, 0xf, true);
  v += __builtin_amdgcn_update_dpp(0, v, 0x114, 0xf, 0xf, true);
  v += __builtin_amdgcn_update_dpp(0, v, 0x118, 0xf, 0xf, true);
  return v;
}
DI int rl(int v, int l) { return __builtin_amdgcn_readlane(v, l); }

template <int W> DI void dma_piece(unsigned so, unsigned m0v, unsigned voff, __amdgpu_buffer_rsrc_t srd) {
  asm volatile("s_mov_b32 m0, %1\n\ts_waitcnt vmcnt(%4)\n\tbuffer_load_dwordx4 %2, %3, %0 offen nt lds"
               :: "s"(so), "s"(m0v), "v"(voff), "s"(srd), "i"(W - 1) : "memory");
}
DI void lds_store(unsigned addr, unsigned val) { asm volatile("ds_write_b32 %0, %1" :: "v"(addr), "v"(val) : "memory"); }

struct Part { int lenv, nb, scnt, sfirst, Pin, R; unsigned segmask; };
DI Part partition(int rawlen, int j) {
  Part p;
  p.lenv = (rawlen <= 0 || rawlen > SKV) ? SKV : rawlen;
  p.nb = (p.lenv + 31) >> 5;
  const int nr = max(p.nb - EXTRA, 1);
  p.Pin = row_scan(nr);
  const int P = p.Pin - nr;
  p.R = rl(p.Pin, 15);
  const int lo = (j * p.R) >> 5, hi = ((j + 1) * p.R) >> 5;
  const int a0 = max(lo, P);
  const int cred = min(hi, p.Pin) - a0;
  const bool starts = (a0 == P);
  p.sfirst = starts ? 0 : (p.nb - nr) + a0 - P;
  p.scnt = cred > 0 ? cred + (starts ? p.nb - nr : 0) : 0;
  p.segmask = (unsigned)__ballot(cred > 0) & 0xffffu;
  return p;
}

DI void gather5(u32x4 (&r)[4], u32x2& ml, unsigned voff, unsigned vml, __amdgpu_buffer_rsrc_t srd, unsigned soff) {
  asm volatile("s_nop 4\n\t"
               "buffer_load_dwordx2 %4, %6, %7, %8 offen sc1\n\t"
               "buffer_load_dwordx4 %0, %5, %7, %8 offen sc1\n\t"
               "buffer_load_dwordx4 %1, %5, %7, %8 offen offset:1024 sc1\n\t"
               "buffer_load_dwordx4 %2, %5, %7, %8 offen offset:2048 sc1\n\t"
               "buffer_load_dwordx4 %3, %5, %7, %8 offen offset:3072 sc1"
               : "+v"(r[0]), "+v"(r[1]), "+v"(r[2]), "+v"(r[3]), "+v"(ml)
               : "v"(voff), "v"(vml), "s"(srd), "s"(soff) : "memory");
}
#define GR_WAIT() asm volatile("s_waitcnt vmcnt(0)" \
    : "+v"(gr[0][0]), "+v"(gr[0][1]), "+v"(gr[0][2]), "+v"(gr[0][3]), "+v"(gml[0]), \
      "+v"(gr[1][0]), "+v"(gr[1][1]), "+v"(gr[1][2]), "+v"(gr[1][3]), "+v"(gml[1]), \
      "+v"(gr[2][0]), "+v"(gr[2][1]), "+v"(gr[2][2]), "+v"(gr[2][3]), "+v"(gml[2]), "+v"(flagv) :: "memory")

#define TILE_LOOP_ASM \
  "s_waitcnt lgkmcnt(0)\n" \
  "v_mov_b32 v200, 0x3f803f80\n" \
  "v_mov_b32 v201, 0x3f803f80\n" \
  "v_mov_b32 v202, 0x3f803f80\n" \
  "v_mov_b32 v203, 0x3f803f80\n" \
  "s_lshl_b32 %[nv], %[tg], 1\n" \
  "s_add_i32 %[nv], %[nv], 2\n" \
  "s_lshl_b32 %[slot], %[tg], 14\n" \
  "s_and_b32 %[slot], %[slot], 0x1c000\n" \
  "s_cmp_eq_u32 %[kr], 0\n" \
  "s_cbranch_scc1 .LpollK_%=\n" \
  ".Ltop_%=:\n" \
  "s_cmp_lt_u32 %[full], %[nv]\n" \
  "s_cbranch_scc1 .LqkL_%=\n" \
  "s_waitcnt lgkmcnt(0)\n" \
  "v_add_u32 v206, %[slot], %[rdv]\n" \
  "v_mfma_f32_16x16x32_bf16 v[180:183], v[116:119], v[100:103], 0\n" \
  "v_xor_b32 v207, 32, v206\n" \
  "ds_read_b64_tr_b16 v[148:149], v206 offset:0\n" \
  "ds_read_b64_tr_b16 v[150:151], v206 offset:4096\n" \
  "v_mfma_f32_16x16x32_bf16 v[180:183], v[120:123], v[104:107], v[180:183]\n" \
  "ds_read_b64_tr_b16 v[152:153], v207 offset:0\n" \
  "ds_read_b64_tr_b16 v[154:155], v207 offset:4096\n" \
  "ds_read_b64_tr_b16 v[156:157], v206 offset:256\n" \
  "v_mfma_f32_16x16x32_bf16 v[180:183], v[124:127], v[108:111], v[180:183]\n" \
  "ds_read_b64_tr_b16 v[158:159], v206 offset:4352\n" \
  "ds_read_b64_tr_b16 v[160:161], v207 offset:256\n" \
  "ds_read_b64_tr_b16 v[162:163], v207 offset:4352\n" \
  "v_mfma_f32_16x16x32_bf16 v[180:183], v[128:131], v[112:115], v[180:183]\n" \
  "ds_read_b64_tr_b16 v[164:165], v206 offset:512\n" \
  "ds_read_b64_tr_b16 v[166:167], v206 offset:4608\n" \
  "ds_read_b64_tr_b16 v[168:169], v207 offset:512\n" \
  "v_mfma_f32_16x16x32_bf16 v[184:187], v[132:135], v[100:103], 0\n" \
  "ds_read_b64_tr_b16 v[170:171], v207 offset:4608\n" \
  "ds_read_b64_tr_b16 v[172:173], v206 offset:768\n" \
  "ds_read_b64_tr_b16 v[174:175], v206 offset:4864\n" \
  "v_mfma_f32_16x16x32_bf16 v[184:187], v[136:139], v[104:107], v[184:187]\n" \
  "ds_read_b64_tr_b16 v[176:177], v207 offset:768\n" \
  "ds_read_b64_tr_b16 v[178:179], v207 offset:4864\n" \
  "s_add_i32 %[tmp], %[tg], 1\n" \
  "v_mfma_f32_16x16x32_bf16 v[184:187], v[140:143], v[108:111], v[184:187]\n" \
  "v_mov_b32 v210, %[tmp]\n" \
  "s_mov_b32 %[vd], 1\n" \
  "ds_write_b32 %[done], v210\n" \
  "v_mfma_f32_16x16x32_bf16 v[184:187], v[144:147], v[112:115], v[184:187]\n" \
  ".Lsm_%=:\n" \
  "s_cmp_ge_i32 %[tg], %[maskat]\n" \
  "s_cbranch_scc1 .Lmask_%=\n" \
  ".Lsm2_%=:\n" \
  "v_fma_f32 v188, v180, %[c], %[nm]\n" \
  "v_fma_f32 v189, v181, %[c], %[nm]\n" \
  "v_fma_f32 v190, v182, %[c], %[nm]\n" \
  "v_fma_f32 v191, v183, %[c], %[nm]\n" \
  "v_maximum3_f32 v208, v180, v181, v182\n" \
  "v_exp_f32 v188, v188\n" \
  "v_exp_f32 v189, v189\n" \
  "v_exp_f32 v190, v190\n" \
  "v_exp_f32 v191, v191\n" \
  "v_fma_f32 v192, v184, %[c], %[nm]\n" \
  "v_fma_f32 v193, v185, %[c], %[nm]\n" \
  "v_fma_f32 v194, v186, %[c], %[nm]\n" \
  "v_fma_f32 v195, v187, %[c], %[nm]\n" \
  "v_maximum3_f32 v208, v208, v183, v184\n" \
  "v_maximum3_f32 v209, v185, v186, v187\n" \
  "v_exp_f32 v192, v192\n" \
  "v_exp_f32 v193, v193\n" \
  "v_exp_f32 v194, v194\n" \
  "v_exp_f32 v195, v195\n" \
  "v_maximum3_f32 v208, v208, v209, v209\n" \
  "v_cvt_pk_bf16_f32 v196, v188, v189\n" \
  "v_cvt_pk_bf16_f32 v197, v190, v191\n" \
  "v_cmp_gt_f32 vcc, v208, %[thr]\n" \
  "v_cvt_pk_bf16_f32 v198, v192, v193\n" \
  "v_cvt_pk_bf16_f32 v199, v194, v195\n" \
  "s_cbranch_vccnz .Lresc_%=\n" \
  ".Lpv_%=:\n" \
  "s_cmp_eq_u32 %[vd], 0\n" \
  "s_cbranch_scc1 .LvL_%=\n" \
  ".Lpv2_%=:\n" \
  "s_add_i32 %[tg], %[tg], 1\n" \
  "s_add_i32 %[slot], %[slot], 0x4000\n" \
  "s_and_b32 %[slot], %[slot], 0x1c000\n" \
  "s_cmp_le_u32 %[full], %[nv]\n" \
  "s_cbranch_scc1 .LpvL_%=\n" \
  "v_mfma_f32_16x16x32_bf16 v[96:99], v[200:203], v[196:199], v[96:99]\n" \
  "v_add_u32 v205, %[slot], %[rdk]\n" \
  "s_add_i32 %[nv], %[nv], 2\n" \
  "s_waitcnt lgkmcnt(0)\n" \
  "v_mfma_f32_16x16x32_bf16 v[64:67], v[148:151], v[196:199], v[64:67]\n" \
  "ds_read_b128 v[116:119], v205 offset:0\n" \
  "ds_read_b128 v[120:123], v205 offset:256\n" \
  "v_mfma_f32_16x16x32_bf16 v[68:71], v[152:155], v[196:199], v[68:71]\n" \
  "ds_read_b128 v[124:127], v205 offset:512\n" \
  "ds_read_b128 v[128:131], v205 offset:768\n" \
  "v_mfma_f32_16x16x32_bf16 v[72:75], v[156:159], v[196:199], v[72:75]\n" \
  "ds_read_b128 v[132:135], v205 offset:4096\n" \
  "ds_read_b128 v[136:139], v205 offset:4352\n" \
  "v_mfma_f32_16x16x32_bf16 v[76:79], v[160:163], v[196:199], v[76:79]\n" \
  "ds_read_b128 v[140:143], v205 offset:4608\n" \
  "ds_read_b128 v[144:147], v205 offset:4864\n" \
  "v_mfma_f32_16x16x32_bf16 v[80:83], v[164:167], v[196:199], v[80:83]\n" \
  "ds_read_b32 v204, %[ctl]\n" \
  "v_mfma_f32_16x16x32_bf16 v[84:87], v[168:171], v[196:199], v[84:87]\n" \
  "v_mfma_f32_16x16x32_bf16 v[88:91], v[172:175], v[196:199], v[88:91]\n" \
  "v_mfma_f32_16x16x32_bf16 v[92:95], v[176:179], v[196:199], v[92:95]\n" \
  "s_waitcnt lgkmcnt(0)\n" \
  "v_readfirstlane_b32 %[full], v204\n" \
  "s_cmp_lt_i32 %[tg], %[stop]\n" \
  "s_cbranch_scc1 .Ltop_%=\n" \
  "s_mov_b32 %[kr], 1\n" \
  "s_branch .Lexit_%=\n" \
  ".LvL_%=:\n" \
  "v_add_u32 v206, %[slot], %[rdv]\n" \
  "s_add_i32 %[tmp], %[tg], 1\n" \
  "v_xor_b32 v207, 32, v206\n" \
  "v_mov_b32 v210, %[tmp]\n" \
  ".LpV_%=:\n" \
  "ds_read_b32 v204, %[ctl]\n" \
  "s_addk_i32 %[spin], -1\n" \
  "s_cmp_lt_i32 %[spin], 0\n" \
  "s_cbranch_scc1 .LpVx_%=\n" \
  "s_waitcnt lgkmcnt(0)\n" \
  "v_readfirstlane_b32 %[full], v204\n" \
  "s_cmp_lt_u32 %[full], %[nv]\n" \
  "s_cbranch_scc1 .LpV_%=\n" \
  ".LpVx_%=:\n" \
  "ds_read_b64_tr_b16 v[148:149], v206 offset:0\n" \
  "ds_read_b64_tr_b16 v[150:151], v206 offset:4096\n" \
  "ds_read_b64_tr_b16 v[152:153], v207 offset:0\n" \
  "ds_read_b64_tr_b16 v[154:155], v207 offset:4096\n" \
  "ds_read_b64_tr_b16 v[156:157], v206 offset:256\n" \
  "ds_read_b64_tr_b16 v[158:159], v206 offset:4352\n" \
  "ds_read_b64_tr_b16 v[160:161], v207 offset:256\n" \
  "ds_read_b64_tr_b16 v[162:163], v207 offset:4352\n" \
  "ds_read_b64_tr_b16 v[164:165], v206 offset:512\n" \
  "ds_read_b64_tr_b16 v[166:167], v206 offset:4608\n" \
  "ds_read_b64_tr_b16 v[168:169], v207 offset:512\n" \
  "ds_read_b64_tr_b16 v[170:171], v207 offset:4608\n" \
  "ds_read_b64_tr_b16 v[172:173], v206 offset:768\n" \
  "ds_read_b64_tr_b16 v[174:175], v206 offset:4864\n" \
  "ds_read_b64_tr_b16 v[176:177], v207 offset:768\n" \
  "ds_read_b64_tr_b16 v[178:179], v207 offset:4864\n" \
  "ds_write_b32 %[done], v210\n" \
  "s_branch .Lpv2_%=\n" \
  ".LpvL_%=:\n" \
  "v_mfma_f32_16x16x32_bf16 v[96:99], v[200:203], v[196:199], v[96:99]\n" \
  "s_add_i32 %[nv], %[nv], 2\n" \
  "s_waitcnt lgkmcnt(15)\n" \
  "v_mfma_f32_16x16x32_bf16 v[64:67], v[148:151], v[196:199], v[64:67]\n" \
  "s_waitcnt lgkmcnt(13)\n" \
  "v_mfma_f32_16x16x32_bf16 v[68:71], v[152:155], v[196:199], v[68:71]\n" \
  "s_waitcnt lgkmcnt(11)\n" \
  "v_mfma_f32_16x16x32_bf16 v[72:75], v[156:159], v[196:199], v[72:75]\n" \
  "s_waitcnt lgkmcnt(9)\n" \
  "v_mfma_f32_16x16x32_bf16 v[76:79], v[160:163], v[196:199], v[76:79]\n" \
  "s_waitcnt lgkmcnt(7)\n" \
  "v_mfma_f32_16x16x32_bf16 v[80:83], v[164:167], v[196:199], v[80:83]\n" \
  "s_waitcnt lgkmcnt(5)\n" \
  "v_mfma_f32_16x16x32_bf16 v[84:87], v[168:171], v[196:199], v[84:87]\n" \
  "s_waitcnt lgkmcnt(3)\n" \
  "v_mfma_f32_16x16x32_bf16 v[88:91], v[172:175], v[196:199], v[88:91]\n" \
  "s_waitcnt lgkmcnt(1)\n" \
  "v_mfma_f32_16x16x32_bf16 v[92:95], v[176:179], v[196:199], v[92:95]\n" \
  "s_mov_b32 %[kr], 0\n" \
  "s_cmp_ge_i32 %[tg], %[stop]\n" \
  "s_cbranch_scc1 .Lexit_%=\n" \
  ".LpollK_%=:\n" \
  "v_add_u32 v205, %[slot], %[rdk]\n" \
  "s_add_i32 %[tmp], %[nv], -1\n" \
  ".LpK_%=:\n" \
  "ds_read_b32 v204, %[ctl]\n" \
  "s_addk_i32 %[spin], -1\n" \
  "s_cmp_lt_i32 %[spin], 0\n" \
  "s_cbranch_scc1 .LpKx_%=\n" \
  "s_waitcnt lgkmcnt(0)\n" \
  "v_readfirstlane_b32 %[full], v204\n" \
  "s_cmp_lt_u32 %[full], %[tmp]\n" \
  "s_cbranch_scc1 .LpK_%=\n" \
  ".LpKx_%=:\n" \
  "ds_read_b128 v[116:119], v205 offset:0\n" \
  "ds_read_b128 v[120:123], v205 offset:256\n" \
  "ds_read_b128 v[124:127], v205 offset:512\n" \
  "ds_read_b128 v[128:131], v205 offset:768\n" \
  "ds_read_b128 v[132:135], v205 offset:4096\n" \
  "ds_read_b128 v[136:139], v205 offset:4352\n" \
  "ds_read_b128 v[140:143], v205 offset:4608\n" \
  "ds_read_b128 v[144:147], v205 offset:4864\n" \
  "s_cmp_ge_u32 %[full], %[nv]\n" \
  "s_cbranch_scc1 .Ltop_%=\n" \
  ".LqkL_%=:\n" \
  "s_waitcnt lgkmcnt(7)\n" \
  "v_mfma_f32_16x16x32_bf16 v[180:183], v[116:119], v[100:103], 0\n" \
  "s_waitcnt lgkmcnt(6)\n" \
  "v_mfma_f32_16x16x32_bf16 v[180:183], v[120:123], v[104:107], v[180:183]\n" \
  "s_waitcnt lgkmcnt(5)\n" \
  "v_mfma_f32_16x16x32_bf16 v[180:183], v[124:127], v[108:111], v[180:183]\n" \
  "s_waitcnt lgkmcnt(4)\n" \
  "v_mfma_f32_16x16x32_bf16 v[180:183], v[128:131], v[112:115], v[180:183]\n" \
  "s_waitcnt lgkmcnt(3)\n" \
  "v_mfma_f32_16x16x32_bf16 v[184:187], v[132:135], v[100:103], 0\n" \
  "s_waitcnt lgkmcnt(2)\n" \
  "v_mfma_f32_16x16x32_bf16 v[184:187], v[136:139], v[104:107], v[184:187]\n" \
  "s_waitcnt lgkmcnt(1)\n" \
  "v_mfma_f32_16x16x32_bf16 v[184:187], v[140:143], v[108:111], v[184:187]\n" \
  "s_waitcnt lgkmcnt(0)\n" \
  "v_mfma_f32_16x16x32_bf16 v[184:187], v[144:147], v[112:115], v[184:187]\n" \
  "s_mov_b32 %[vd], 0\n" \
  "s_branch .Lsm_%=\n" \
  ".Lmask_%=:\n" \
  "s_lshl_b32 %[tmp], %[tg], 5\n" \
  "s_sub_i32 %[tmp], %[limb], %[tmp]\n" \
  "v_sub_u32 v208, %[tmp], %[g4]\n" \
  "v_mov_b32 v209, 0xff800000\n" \
  "s_nop 7\n" \
  "v_cmp_lt_i32 vcc, 0, v208\n" \
  "s_nop 1\n" \
  "v_cndmask_b32 v180, v209, v180, vcc\n" \
  "v_cmp_lt_i32 vcc, 1, v208\n" \
  "s_nop 1\n" \
  "v_cndmask_b32 v181, v209, v181, vcc\n" \
  "v_cmp_lt_i32 vcc, 2, v208\n" \
  "s_nop 1\n" \
  "v_cndmask_b32 v182, v209, v182, vcc\n" \
  "v_cmp_lt_i32 vcc, 3, v208\n" \
  "s_nop 1\n" \
  "v_cndmask_b32 v183, v209, v183, vcc\n" \
  "v_cmp_lt_i32 vcc, 16, v208\n" \
  "s_nop 1\n" \
  "v_cndmask_b32 v184, v209, v184, vcc\n" \
  "v_cmp_lt_i32 vcc, 17, v208\n" \
  "s_nop 1\n" \
  "v_cndmask_b32 v185, v209, v185, vcc\n" \
  "v_cmp_lt_i32 vcc, 18, v208\n" \
  "s_nop 1\n" \
  "v_cndmask_b32 v186, v209, v186, vcc\n" \
  "v_cmp_lt_i32 vcc, 19, v208\n" \
  "s_nop 1\n" \
  "v_cndmask_b32 v187, v209, v187, vcc\n" \
  "s_cmp_gt_i32 %[zq], 0\n" \
  "s_cbranch_scc1 .Lsm2_%=\n" \
  "v_mov_b32 v180, 0\n" \
  "v_mov_b32 v181, 0\n" \
  "v_mov_b32 v182, 0\n" \
  "v_mov_b32 v183, 0\n" \
  "v_mov_b32 v184, 0\n" \
  "v_mov_b32 v185, 0\n" \
  "v_mov_b32 v186, 0\n" \
  "v_mov_b32 v187, 0\n" \
  "s_branch .Lsm2_%=\n" \
  ".Lresc_%=:\n" \
  "v_mov_b32 v209, v208\n" \
  "s_nop 1\n" \
  "v_permlane16_swap_b32 v208, v209\n" \
  "s_nop 1\n" \
  "v_maximum3_f32 v208, v208, v209, v209\n" \
  "v_mov_b32 v209, v208\n" \
  "s_nop 1\n" \
  "v_permlane32_swap_b32 v208, v209\n" \
  "s_nop 1\n" \
  "v_maximum3_f32 v208, v208, v209, %[mr]\n" \
  "v_sub_f32 v209, %[mr], v208\n" \
  "v_mul_f32 v209, %[c], v209\n" \
  "v_exp_f32 v209, v209\n" \
  "v_mov_b32 %[mr], v208\n" \
  "v_mul_f32 %[nm], %[c], v208\n" \
  "v_add_f32 %[thr], %[thc], v208\n" \
  "v_xor_b32 %[nm], 0x80000000, %[nm]\n" \
  "v_mul_f32 v64, v209, v64\n" \
  "v_mul_f32 v65, v209, v65\n" \
  "v_mul_f32 v66, v209, v66\n" \
  "v_mul_f32 v67, v209, v67\n" \
  "v_mul_f32 v68, v209, v68\n" \
  "v_mul_f32 v69, v209, v69\n" \
  "v_mul_f32 v70, v209, v70\n" \
  "v_mul_f32 v71, v209, v71\n" \
  "v_mul_f32 v72, v209, v72\n" \
  "v_mul_f32 v73, v209, v73\n" \
  "v_mul_f32 v74, v209, v74\n" \
  "v_mul_f32 v75, v209, v75\n" \
  "v_mul_f32 v76, v209, v76\n" \
  "v_mul_f32 v77, v209, v77\n" \
  "v_mul_f32 v78, v209, v78\n" \
  "v_mul_f32 v79, v209, v79\n" \
  "v_mul_f32 v80, v209, v80\n" \
  "v_mul_f32 v81, v209, v81\n" \
  "v_mul_f32 v82, v209, v82\n" \
  "v_mul_f32 v83, v209, v83\n" \
  "v_mul_f32 v84, v209, v84\n" \
  "v_mul_f32 v85, v209, v85\n" \
  "v_mul_f32 v86, v209, v86\n" \
  "v_mul_f32 v87, v209, v87\n" \
  "v_mul_f32 v88, v209, v88\n" \
  "v_mul_f32 v89, v209, v89\n" \
  "v_mul_f32 v90, v209, v90\n" \
  "v_mul_f32 v91, v209, v91\n" \
  "v_mul_f32 v92, v209, v92\n" \
  "v_mul_f32 v93, v209, v93\n" \
  "v_mul_f32 v94, v209, v94\n" \
  "v_mul_f32 v95, v209, v95\n" \
  "v_mul_f32 v96, v209, v96\n" \
  "v_mul_f32 v97, v209, v97\n" \
  "v_mul_f32 v98, v209, v98\n" \
  "v_mul_f32 v99, v209, v99\n" \
  "v_fma_f32 v188, v180, %[c], %[nm]\n" \
  "v_fma_f32 v189, v181, %[c], %[nm]\n" \
  "v_fma_f32 v190, v182, %[c], %[nm]\n" \
  "v_fma_f32 v191, v183, %[c], %[nm]\n" \
  "v_fma_f32 v192, v184, %[c], %[nm]\n" \
  "v_fma_f32 v193, v185, %[c], %[nm]\n" \
  "v_fma_f32 v194, v186, %[c], %[nm]\n" \
  "v_fma_f32 v195, v187, %[c], %[nm]\n" \
  "v_exp_f32 v188, v188\n" \
  "v_exp_f32 v189, v189\n" \
  "v_exp_f32 v190, v190\n" \
  "v_exp_f32 v191, v191\n" \
  "v_exp_f32 v192, v192\n" \
  "v_exp_f32 v193, v193\n" \
  "v_exp_f32 v194, v194\n" \
  "v_exp_f32 v195, v195\n" \
  "s_nop 0\n" \
  "v_cvt_pk_bf16_f32 v196, v188, v189\n" \
  "v_cvt_pk_bf16_f32 v197, v190, v191\n" \
  "v_cvt_pk_bf16_f32 v198, v192, v193\n" \
  "v_cvt_pk_bf16_f32 v199, v194, v195\n" \
  "s_branch .Lpv_%=\n" \
  ".Lexit_%=:\n" \
  "s_nop 7\n"
#define TILE_LOOP_CLOB "v148", "v149", "v150", "v151", "v152", "v153", "v154", "v155", "v156", "v157", "v158", "v159", "v160", "v161", "v162", "v163", "v164", "v165", "v166", "v167", "v168", "v169", "v170", "v171", "v172", "v173", "v174", "v175", "v176", "v177", "v178", "v179", "v180", "v181", "v182", "v183", "v184", "v185", "v186", "v187", "v188", "v189", "v190", "v191", "v192", "v193", "v194", "v195", "v196", "v197", "v198", "v199", "v200", "v201", "v202", "v203", "v204", "v205", "v206", "v207", "v208", "v209", "v210"

__global__ void __launch_bounds__(320, 2)
attn_paged(const __bf16* __restrict__ q, const __bf16* __restrict__ kc, const __bf16* __restrict__ vc,
           const int* __restrict__ bt, const int* __restrict__ lens, __bf16* __restrict__ out, char* ws, unsigned kv_bytes) {
  STAMP(0);
  const int tid = threadIdx.x, lane = tid & 63;
  const int w = __builtin_amdgcn_readfirstlane(tid >> 6);
  const int g = lane >> 4, i = lane & 15;
  const int h = blockIdx.x & 7, j = blockIdx.x >> 3;
  unsigned codew = 0;
  u64 pc0;
  asm volatile("s_getpc_b64 %0" : "=s"(pc0));
  const GAS unsigned* codep = (const GAS unsigned*)(pc0 & ~255ull) + min((j * 4 + (lane & 3)) * 128, CODE_BYTES - 4) / 4;
  if (w == 0) { if (lane < 4) codew = *codep; (void)0; }
  if (w == 4) {
    lds_store(CTL_OFF + 4 * lane, 0u);
    lds_store(CTL_OFF + 256 + 4 * lane, 0u);
  }
  asm volatile("" : "+s"(q), "+s"(kc), "+s"(vc), "+s"(bt), "+s"(lens), "+s"(out), "+s"(ws), "+s"(kv_bytes));
  const int rawlen = ((const GAS int*)lens)[i];
  const int r0 = min(max((j >> 1) - 1, 0), 13);
  if (w == 4) {
    const __amdgpu_buffer_rsrc_t srdB = __builtin_amdgcn_make_buffer_rsrc((void*)bt, (short)0, 16 * NBPS * 4, 0x00020000);
    asm volatile("s_mov_b32 m0, %0\n\ts_nop 0\n\t"
                 "buffer_load_dwordx4 %1, %2, %3 offen lds\n\t"
                 "buffer_load_dwordx4 %1, %2, %3 offen offset:1024 lds\n\t"
                 "buffer_load_dwordx4 %1, %2, %3 offen offset:2048 lds"
                 :: "s"(BT_OFF), "v"(lane * 16), "s"(srdB), "s"(r0 * 1024) : "memory");
  }
  asm volatile("s_waitcnt lgkmcnt(0)\n\ts_barrier" ::: "memory");

  if (__builtin_expect(w == 4, 1)) {
    __builtin_amdgcn_s_setprio(3);
    const __amdgpu_buffer_rsrc_t srdK =
        __builtin_amdgcn_make_buffer_rsrc((void*)((const char*)kc + h * 256), (short)0, kv_bytes - h * 256, 0x00020000);
    const __amdgpu_buffer_rsrc_t srdV =
        __builtin_amdgcn_make_buffer_rsrc((void*)((const char*)vc + h * 256), (short)0, kv_bytes - h * 256, 0x00020000);
    unsigned vK[4], vV[2];
    const unsigned vb = ((lane >> 2) & 3) * 2048 + g * 64;
#pragma unroll
    for (int p = 0; p < 4; ++p) vK[p] = p * 8192 + vb + 16 * ((lane & 3) ^ p);
#pragma unroll
    for (int p = 0; p < 2; ++p) vV[p] = p * 8192 + vb + 16 * ((lane & 3) ^ (2 * p));
    asm volatile("" : "+v"(vK[0]), "+v"(vK[1]), "+v"(vK[2]), "+v"(vK[3]), "+v"(vV[0]), "+v"(vV[1]));
    STAMP(5);
    const Part pt = partition(rawlen, j);
    STAMP(6);
    int n = 0;
    int off0 = 0, off1 = 0, last0 = 0, last1 = 0;
    for (unsigned m = pt.segmask; m; m &= m - 1) {
      const int b = __builtin_ctz(m);
      const int f = rl(pt.sfirst, b) - n, lp = b * NBPS + ((rl(pt.lenv, b) + 15) >> 4) - 1;
      const bool c0 = lane >= n, c1 = lane + 64 >= n;
      off0 = c0 ? b * NBPS + 2 * min(f + lane, 127) : off0; last0 = c0 ? lp : last0;
      off1 = c1 ? b * NBPS + 2 * min(f + lane + 64, 127) : off1; last1 = c1 ? lp : last1;
      n += rl(pt.scnt, b);
    }
    i32x2 pg0, pg1;
    if (__builtin_expect((pt.segmask & ~(7u << r0)) == 0, 1)) {
      asm volatile("s_waitcnt vmcnt(0)" ::: "memory");
      pg0 = *(const LAS i32x2*)(size_t)(unsigned)(BT_OFF + 4 * max(off0 - r0 * NBPS, 0));
      pg1 = *(const LAS i32x2*)(size_t)(unsigned)(BT_OFF + 4 * max(off1 - r0 * NBPS, 0));
    } else {
      pg0 = *(const GAS i32x2*)((const GAS int*)bt + off0);
      pg1 = *(const GAS i32x2*)((const GAS int*)bt + off1);
    }
    unsigned oA = (unsigned)pg0[0] << 15, oB = (unsigned)(off0 < last0 ? pg0[1] : pg0[0]) << 15;
    unsigned oA1 = (unsigned)pg1[0] << 15, oB1 = (unsigned)(off1 < last1 ? pg1[1] : pg1[0]) << 15;
    asm volatile("" : "+v"(oA), "+v"(oB), "+v"(oA1), "+v"(oB1));
    const unsigned fulladdr = CTL_OFF + 4 * lane;
    STAMP(1);
    int freed = 0;
    for (int t = 0; t < n; ++t) {
      if (t == 64) { oA = oA1; oB = oB1; }
      unsigned a = rl(oA, t & 63), b = rl(oB, t & 63);
      for (unsigned spins = 0; freed < t - NSLOT + 1 && spins < SPIN_CAP; ++spins) {
        u32x4 d;
        asm volatile("ds_read_b128 %0, %1\n\ts_waitcnt lgkmcnt(0)" : "=v"(d) : "v"(CTL_OFF + 256) : "memory");
        freed = __builtin_amdgcn_readfirstlane(min(min(d[0], d[1]), min(d[2], d[3])));
      }
      asm volatile("s_nop 4" : "+s"(a), "+s"(b));
      const unsigned mb = (t & (NSLOT - 1)) * SLOT_BYTES;
#pragma unroll
      for (int u = 0; u < 16; ++u) {
        const unsigned so = ((u & 4) ? b : a) + ((u & 8) ? (u & 2) * 8192 : 0);
        dma_piece<WIN>(so, mb + u * 1024, (u & 8) ? vV[u & 1] : vK[u & 3], (u & 8) ? srdV : srdK);
        if (((u + 1 - WIN) & 7) == 0) lds_store(fulladdr, (unsigned)max((16 * t + u + 1 - WIN) >> 3, 0));
      }
    }
    STAMP(2);
    if (WIN > 24) { asm volatile("s_waitcnt vmcnt(24)" ::: "memory"); lds_store(fulladdr, (unsigned)max(2 * n - 3, 0)); }
    asm volatile("s_waitcnt vmcnt(16)" ::: "memory");
    lds_store(fulladdr, (unsigned)max(2 * n - 2, 0));
    asm volatile("s_waitcnt vmcnt(8)" ::: "memory");
    lds_store(fulladdr, (unsigned)(2 * n - 1));
    asm volatile("s_waitcnt vmcnt(0)" ::: "memory");
    lds_store(fulladdr, (unsigned)(2 * n));
    STAMP(3);
    STAMP_FLUSH(0, 8);
    return;
  }

  GAS u64* ep = (GAS u64*)(ws + WS_EPOCH) + (h * 4 + w) * 32 + (lane & 31);
  const u64 tagv = __hip_atomic_load(ep, RLX_AGENT) + TAG_STEP;
  const Part pt = partition(rawlen, j);
  if (pt.segmask == 0) return;
  const int xg = (g >> 1) ^ (3 * (g & 1));
  const unsigned rdK = (i >> 2) * 1024 + (i & 3) * 64 + 16 * ((i >> 2) ^ xg);
  const unsigned rdV = 8192 + g * 1024 + (i >> 2) * 64 + 32 * (g & 1) + 8 * (i & 3);
  const unsigned doneaddr = CTL_OFF + 256 + 4 * w, ctladdr = CTL_OFF;
  const __amdgpu_buffer_rsrc_t srdP = __builtin_amdgcn_make_buffer_rsrc((void*)(ws + WS_PART), (short)0, PART_TOTAL, 0x00020000);
  const GAS __bf16* qbase = (const GAS __bf16*)q + ((size_t)(h * 4 + w) * SQ + i) * D_ + 8 * xg;
  const int ntot = rl(row_scan(pt.scnt), 15);

  u32x4 gr[3][4];
  u32x2 gml[3];
  u64 flagv = 0;
#pragma unroll
  for (int e = 0; e < 4; ++e) gr[0][e] = gr[2][e] = u32x4{0u, 0u, 0u, 0u};
  gml[0] = gml[1] = gml[2] = u32x2{0u, 0u};
#pragma unroll
  for (int s = 0; s < 4; ++s) gr[1][s] = *(const GAS u32x4*)(qbase + (size_t)__builtin_ctz(pt.segmask) * HQ * SQ * D_ + 32 * s);

  unsigned hmask;
  {
    const int bl = 31 - __builtin_clz(pt.segmask);
    const int Pe = rl(pt.Pin, bl), k = lane & 31;
    const bool later = k > j && ((k * pt.R) >> 5) < Pe && (((k + 1) * pt.R) >> 5) > ((k * pt.R) >> 5);
    hmask = rl(pt.sfirst, bl) == 0 ? (unsigned)__ballot(later) : 0u;
  }
  const u32x2 mytag = {(unsigned)rl((int)(unsigned)tagv, j), (unsigned)rl((int)(unsigned)(tagv >> 32), j)};
  const unsigned pbase = ((h * 32 + j) * 4 + w) * PART_BYTES;
  const bool mine = lane < 32 && ((hmask >> lane) & 1);
  const unsigned flagoff = mine ? ((h * 32 + lane) * 4 + w) * PART_BYTES + FLAG_OFF : PART_TOTAL;
  int st_pub = 0;
  int st_get = hmask ? 0 : 2;
  auto publish = [&]() __attribute__((always_inline)) {
#pragma unroll
    for (int e = 0; e < 4; ++e) __builtin_amdgcn_raw_buffer_store_b128(gr[2][e], srdP, pbase + lane * 16 + e * 1024, 0, 16);
    __builtin_amdgcn_raw_buffer_store_b64(gml[2], srdP, lane < 16 ? pbase + ML_OFF + lane * 8 : PART_TOTAL, 0, 16);
  };
  auto post_flag = [&]() __attribute__((always_inline)) {
    asm volatile("s_waitcnt vmcnt(0)" ::: "memory");
    __builtin_amdgcn_raw_buffer_store_b64(mytag, srdP, lane == 0 ? pbase + FLAG_OFF : PART_TOTAL, 0, 16);
  };
  auto poll = [&]() __attribute__((always_inline)) {
    asm volatile("buffer_load_dwordx2 %0, %1, %2, 0 offen sc1" : "+v"(flagv) : "v"(flagoff), "s"(srdP) : "memory");
  };
  auto flags_ok = [&]() __attribute__((always_inline)) -> bool {
    asm volatile("s_waitcnt vmcnt(0)" : "+v"(flagv) :: "memory");
    return !__any(mine && flagv != tagv);
  };
  auto gather = [&](unsigned hm) __attribute__((always_inline)) {
#pragma unroll
    for (int c = 0; c < 3; ++c) {
      const unsigned so = hm ? ((h * 32 + __builtin_ctz(hm)) * 4 + w) * PART_BYTES : PART_TOTAL;
      gather5(gr[c], gml[c], lane * 16, ML_OFF + i * 8, srdP, so);
      hm &= hm - 1;
    }
  };
  const int ev_at = ntot - PUB_EARLY;

  int tg = 0;
  unsigned full = 0;
  int kready = 0, spin = (int)SPIN_CAP;
  u32x32 kreg;
#pragma unroll
  for (int e = 0; e < 32; ++e) kreg[e] = 0u;
  asm volatile("" :: "v"(codew));
  STAMP(1);

  f32x32 acc;
  float mrun, lrun;
  int b;
  bool cont;
  auto park = [&]() __attribute__((always_inline)) {
#pragma unroll
    for (int c = 0; c < 4; ++c)
      gr[2][c] = u32x4{cvtpk(acc[8 * c], acc[8 * c + 1]), cvtpk(acc[8 * c + 2], acc[8 * c + 3]), cvtpk(acc[8 * c + 4], acc[8 * c + 5]), cvtpk(acc[8 * c + 6], acc[8 * c + 7])};
    gml[2] = u32x2{__float_as_uint(mrun), __float_as_uint(lrun)};
  };
  auto write_out = [&]() __attribute__((always_inline)) {
    const float inv = __builtin_amdgcn_rcpf(lrun);
    const __amdgpu_buffer_rsrc_t srdO = __builtin_amdgcn_make_buffer_rsrc((void*)out, (short)0, 16 * HQ * SQ * D_ * 2, 0x00020000);
    const unsigned obase = ((b * HQ + h * 4 + w) * SQ) * D_ * 2;
    const unsigned ovoff = i * 256 + (g >> 1) * 16 + (g & 1) * 32;
#pragma unroll
    for (int e = 0; e < 8; e += 2) {
      auto s0 = __builtin_amdgcn_permlane16_swap(cvtpk(acc[4 * e] * inv, acc[4 * e + 1] * inv), cvtpk(acc[4 * e + 4] * inv, acc[4 * e + 5] * inv), false, false);
      auto s1 = __builtin_amdgcn_permlane16_swap(cvtpk(acc[4 * e + 2] * inv, acc[4 * e + 3] * inv), cvtpk(acc[4 * e + 6] * inv, acc[4 * e + 7] * inv), false, false);
      __builtin_amdgcn_raw_buffer_store_b128(u32x4{s0[0], s1[0], s0[1], s1[1]}, srdO, obase + ovoff + e * 32, 0, OUT_AUX);
    }
  };
  for (unsigned m = pt.segmask;;) {
    b = __builtin_ctz(m);
    m &= m - 1;
    const int first = rl(pt.sfirst, b), cnt = rl(pt.scnt, b), len = rl(pt.lenv, b);
    const int rawb = rl(rawlen, b);
    const bool zq = rawb <= 0;
    u32x16 qf;
#pragma unroll
    for (int e = 0; e < 16; ++e) qf[e] = gr[1][e >> 2][e & 3];
    asm volatile("" : "+{v[100:115]}"(qf));
    if (m) {
#pragma unroll
      for (int s = 0; s < 4; ++s) gr[1][s] = *(const GAS u32x4*)(qbase + (size_t)__builtin_ctz(m) * HQ * SQ * D_ + 32 * s);
    }
#pragma unroll
    for (int e = 0; e < 32; ++e) acc[e] = 0.f;
    f32x4 lacc = {0.f, 0.f, 0.f, 0.f};
    float mr = -1e30f, nm = 1e30f, thr = -1e30f;
    const int mask_at = zq ? 0 : tg + ((len >> 5) - first);
    const int limb = len - 32 * (first - tg);
    const int segend = tg + cnt;
    do {
      if (__builtin_expect(tg >= ev_at, 0)) {
        if (st_pub == 2) { post_flag(); st_pub = 0; }
        if (st_pub == 1) { publish(); st_pub = 2; }
        if (!m && tg >= ntot - POLL_EARLY) {
          if (st_get == 1) { if (flags_ok()) { gather(hmask); st_get = 2; } else st_get = 0; }
          if (st_get == 0) { poll(); st_get = 1; }
        }
      }
      const bool idle = st_pub == 0 && st_get == 2;
      const int stop = min(segend, idle ? segend : max(ev_at, tg + 1));
      unsigned nv, slot, tmp, vd;
      asm volatile(TILE_LOOP_ASM
                   : "+{v[64:95]}"(acc), "+{v[96:99]}"(lacc), "+{v[116:147]}"(kreg), [mr] "+v"(mr), [nm] "+v"(nm), [thr] "+v"(thr),
                     [tg] "+s"(tg), [full] "+s"(full), [kr] "+s"(kready), [spin] "+s"(spin),
                     [nv] "=&s"(nv), [slot] "=&s"(slot), [tmp] "=&s"(tmp), [vd] "=&s"(vd)
                   : "{v[100:115]}"(qf), [rdk] "v"(rdK), [rdv] "v"(rdV), [ctl] "v"(ctladdr), [done] "v"(doneaddr), [g4] "v"(4 * g),
                     [stop] "s"(stop), [maskat] "s"(mask_at), [limb] "s"(limb), [zq] "s"(rawb), [c] "s"(SCALE_LOG2E), [thc] "s"(THR / SCALE_LOG2E)
                   : "memory", "vcc", "scc", TILE_LOOP_CLOB);
    } while (tg < segend);
    mrun = -nm;
    GR_WAIT();

    lrun = lacc[0];
    if (!m) { cont = first != 0; break; }
    if (first != 0) { park(); st_pub = 1; }
    else write_out();
  }
  STAMP(2);
  if (cont) {
    park();
    publish();
    post_flag();
    STAMP(3);
  } else {
    if (st_pub == 2) post_flag();
    if (st_get != 2) {
#pragma nounroll
      for (unsigned spins = 0; spins < SPIN_CAP; ++spins) {
        if (st_get == 1 && flags_ok()) break;
        poll();
        st_get = 1;
      }
    }
    STAMP(3);
    for (unsigned hm = hmask; hm;) {
      unsigned hn = hm;
      hn &= hn - 1; hn &= hn - 1; hn &= hn - 1;
      if (st_get != 2) { gather(hm); GR_WAIT(); }
      st_get = 0;
      STAMP(4);
      const int np = __builtin_popcount(hm);
      const float m0 = __uint_as_float(gml[0][0]);
      const float m1 = np > 1 ? __uint_as_float(gml[1][0]) : -1e30f, m2 = np > 2 ? __uint_as_float(gml[2][0]) : -1e30f;
      const float mn = fmax2(fmax2(mrun, m0), fmax2(m1, m2));
      const float co = __builtin_amdgcn_exp2f(mrun - mn);
      const float cf[3] = {__builtin_amdgcn_exp2f(m0 - mn), __builtin_amdgcn_exp2f(m1 - mn), __builtin_amdgcn_exp2f(m2 - mn)};
      mrun = mn;
      lrun *= co;
      acc *= co;
#pragma unroll
      for (int c = 0; c < 3; ++c) {
        lrun = fmaf(__uint_as_float(gml[c][1]), cf[c], lrun);
#pragma unroll
        for (int x = 0; x < 16; ++x) {
          const unsigned u = gr[c][x >> 2][x & 3];
          acc[2 * x] = fmaf(__uint_as_float(u << 16), cf[c], acc[2 * x]);
          acc[2 * x + 1] = fmaf(__uint_as_float(u & 0xffff0000u), cf[c], acc[2 * x + 1]);
        }
      }
      hm = hn;
    }
    STAMP(5);
    write_out();
    if (mine) __hip_atomic_store(ep, tagv, RLX_AGENT);
  }
}

extern "C" void kernel_launch(void* const* d_in, const int* in_sizes, int n_in, void* d_out, int out_size, void* d_ws,
                              size_t ws_size, hipStream_t stream) {
  const unsigned kv_bytes = (unsigned)in_sizes[1] * 2u;
  (void)hipFuncSetAttribute((const void*)attn_paged, hipFuncAttributeMaxDynamicSharedMemorySize, LDS_BYTES);
  attn_paged<<<256, 320, LDS_BYTES, stream>>>((const __bf16*)d_in[0], (const __bf16*)d_in[1], (const __bf16*)d_in[2],
                                              (const int*)d_in[3], (const int*)d_in[4], (__bf16*)d_out, (char*)d_ws, kv_bytes);
}
```

```cpp
#include <hip/hip_runtime.h>
#include <hip/hip_bf16.h>
#include <stdint.h>

typedef __bf16 bf16x2 __attribute__((ext_vector_type(2)));
typedef float f32x2 __attribute__((ext_vector_type(2)));
typedef float f32x4 __attribute__((ext_vector_type(4)));
typedef float f32x32 __attribute__((ext_vector_type(32)));
typedef unsigned u32x2 __attribute__((ext_vector_type(2)));
typedef unsigned u32x4 __attribute__((ext_vector_type(4)));
typedef unsigned u32x16 __attribute__((ext_vector_type(16)));
typedef unsigned u32x32 __attribute__((ext_vector_type(32)));
typedef unsigned long long u64;
#define LAS __attribute__((address_space(3)))
#define GAS __attribute__((address_space(1)))
#define DI __device__ __forceinline__

constexpr int HQ = 32, SQ = 16, D_ = 128, SKV = 4096;
constexpr int NSLOT = 8, SLOT_BYTES = 16384;
constexpr int BT_OFF = NSLOT * SLOT_BYTES;
constexpr int JUNK_OFF = BT_OFF + 3072;
constexpr int CTL_OFF = JUNK_OFF + 4096;
constexpr int DONE_OFF = CTL_OFF + 256;
constexpr int LDS_BYTES = CTL_OFF + 512;
static_assert(LDS_BYTES > 80 * 1024 && LDS_BYTES <= 160 * 1024, "one workgroup per CU");
constexpr unsigned REC = 4096 + 256, WGREC = 4 * REC, PART_TOTAL = 8 * 48 * WGREC;
constexpr u64 TAG_STEP = 0x9E3779B97F4A7C15ull;
constexpr int SPIN_CAP = 1 << 22;
constexpr float SCALE_LOG2E = 0.08838834764831845f * 1.4426950408889634f;
constexpr int XC = 40;
constexpr int TAU = 920;
constexpr int LEAD = 400, COLD = 700, SLOW = 46;
constexpr int FB_MIN = 1000, FB_ADD = 150;
constexpr int TOUCH = 3;

DI unsigned cvtpk(float lo, float hi) {
  f32x2 v = {lo, hi};
  return __builtin_bit_cast(unsigned, __builtin_convertvector(v, bf16x2));
}
DI int row_scan(int v) {
  v += __builtin_amdgcn_update_dpp(0, v, 0x111, 0xf, 0xf, true);
  v += __builtin_amdgcn_update_dpp(0, v, 0x112, 0xf, 0xf, true);
  v += __builtin_amdgcn_update_dpp(0, v, 0x114, 0xf, 0xf, true);
  v += __builtin_amdgcn_update_dpp(0, v, 0x118, 0xf, 0xf, true);
  return v;
}
DI int rl(int v, int l) { return __builtin_amdgcn_readlane(v, l); }

struct Part { int scnt, sfirst, lo, rem, R; unsigned segmask; };
DI int cut_at(int k, int R, int lo, int rem) { return min(max((((k * R + 32) >> 4) - lo) >> 1, 0), rem); }
DI Part partition(unsigned lm1  , int idx, int i) {
  Part p;
  const int nb = (lm1 >> 5) + 1;
  const int x = min((rl(row_scan(nb), 15) + XC) >> 5, nb);
  p.rem = nb - x;
  const int pin = row_scan(2 * p.rem + min(p.rem, 1));
  p.lo = pin - 2 * p.rem;
  p.R = rl(pin, 15) - 1;
  if (idx < 16) {
    p.sfirst = 0;
    p.scnt = i == idx ? x : 0;
    p.segmask = 1u << idx;
  } else {
    const int c0 = cut_at(idx - 16, p.R, p.lo, p.rem), c1 = cut_at(idx - 15, p.R, p.lo, p.rem);
    p.scnt = c1 - c0;
    p.sfirst = x + c0;
    p.segmask = (unsigned)__ballot(p.scnt > 0) & 0xffffu;
  }
  return p;
}
DI unsigned helpers_of(const Part& pt, int b, int i) {
  const int lo = rl(pt.lo, b), rem = rl(pt.rem, b);
  return (unsigned)__ballot(cut_at(i + 1, pt.R, lo, rem) > cut_at(i, pt.R, lo, rem)) & 0xffffu;
}

#define LOADER_ASM \
  "v_readfirstlane_b32 s87, %[tid]\n" \
  "s_cmp_ge_u32 s87, 0x100\n" \
  "s_cbranch_scc1 .Ldgo_%=\n" \
  "s_cmp_ge_u32 s87, 64\n" \
  "s_cbranch_scc1 .Ldskip_%=\n" \
    \
  "s_getpc_b64 s[40:41]\n" \
  ".Ldpc_%=:\n" \
  "s_lshr_b32 s71, %[wg], 3\n" \
  "s_lshl_b32 s71, s71, 9\n" \
  "v_and_b32 v1, 3, %[tid]\n" \
  "v_lshl_add_u32 v1, v1, 7, s71\n" \
  "s_mov_b32 m0, %[junk]\n" \
  "v_min_u32 v1, .Lattn_last-.Ldpc_%=, v1\n" \
  "global_load_lds_dword v1, s[40:41]\n" \
  "s_branch .Ldskip_%=\n" \
    \
  ".Ldgo_%=:\n" \
  "s_load_dwordx16 s[40:55], %[ka], 0x0\n" \
  "s_memrealtime s[68:69]\n" \
  "s_setprio 3\n" \
  "s_and_b32 s70, %[wg], 7\n" \
  "s_lshr_b32 s71, %[wg], 3\n" \
  "v_and_b32 v1, 63, %[tid]\n" \
  "v_lshlrev_b32 v7, 2, v1\n" \
  "v_mov_b32 v17, 0\n" \
  "v_add_u32 v7, %[ctl], v7\n" \
  "ds_write_b32 v7, v17\n" \
  "ds_write_b32 v7, v17 offset:256\n" \
  "v_and_b32 v22, 15, v1\n" \
  "v_lshlrev_b32 v18, 2, v22\n" \
  "v_lshlrev_b32 v9, 4, v1\n" \
    \
  "s_sub_i32 s72, s71, 17\n" \
  "s_max_i32 s72, s72, 0\n" \
  "s_min_i32 s72, s72, 13\n" \
  "s_cmp_lt_u32 s71, 16\n" \
  "s_cselect_b32 s72, s71, s72\n" \
  "s_cselect_b32 s75, 1, 0\n" \
  "s_lshl_b32 s88, s72, 10\n" \
  "s_mov_b32 m0, %[bt]\n" \
  "s_movk_i32 s58, 0x4000\n" \
  "s_mov_b32 s59, 0x20000\n" \
  "s_waitcnt lgkmcnt(0)\n" \
  "global_load_dword v10, v18, s[48:49]\n" \
  "s_memrealtime s[50:51]\n" \
  "s_mov_b32 s56, s46\n" \
  "s_and_b32 s57, s47, 0xffff\n" \
  "s_cmp_eq_u32 s75, 1\n" \
  "buffer_load_dwordx4 v9, s[56:59], s88 offen lds\n" \
  "s_cbranch_scc1 .Ldonerow_%=\n" \
  "buffer_load_dwordx4 v9, s[56:59], s88 offen offset:1024 lds\n" \
  "buffer_load_dwordx4 v9, s[56:59], s88 offen offset:2048 lds\n" \
  ".Ldonerow_%=:\n" \
  "s_barrier\n" \
    \
  "s_lshl_b32 s87, s70, 8\n" \
  "s_add_u32 s60, s42, s87\n" \
  "s_addc_u32 s61, s43, 0\n" \
  "s_and_b32 s61, s61, 0xffff\n" \
  "s_sub_u32 s62, s54, s87\n" \
  "s_mov_b32 s63, 0x20000\n" \
  "s_add_u32 s64, s44, s87\n" \
  "s_addc_u32 s65, s45, 0\n" \
  "s_and_b32 s65, s65, 0xffff\n" \
  "s_sub_u32 s66, s54, s87\n" \
  "s_mov_b32 s67, 0x20000\n" \
    \
  "v_bfe_u32 v17, v1, 2, 2\n" \
  "v_lshlrev_b32 v17, 11, v17\n" \
  "v_lshrrev_b32 v18, 4, v1\n" \
  "v_lshl_or_b32 v17, v18, 6, v17\n" \
  "v_and_b32 v18, 48, v9\n" \
  "v_or_b32 v2, v17, v18\n" \
  "v_xor_b32 v3, 0x2010, v2\n" \
  "v_xor_b32 v4, 0x4020, v2\n" \
  "v_xor_b32 v5, 0x6030, v2\n" \
  "v_xor_b32 v6, 0x2020, v2\n" \
  "v_mov_b32 v8, %[done]\n" \
  "v_mov_b32 v12, -2\n" \
  "s_mov_b32 s73, s71\n" \
  "s_mov_b32 s74, 0\n" \
  "s_mov_b32 s76, 0\n" \
  "s_mov_b32 s77, 0\n" \
  "s_mov_b32 s80, 0\n" \
  "s_mov_b32 s82, 0\n" \
  "s_mov_b32 s90, 0\n" \
  "s_mov_b32 s81, %[cap]\n" \
  "s_mov_b32 s91, -1\n" \
  "s_add_u32 s52, s52, %[rep]\n" \
  "s_addc_u32 s53, s53, 0\n" \
    \
  "s_waitcnt lgkmcnt(0)\n" \
  "s_sub_u32 s55, s50, s68\n" \
  "s_cmp_gt_u32 s55, %[slow]\n" \
  "s_cselect_b32 s55, 2 * %[cold], 0\n" \
  "s_lshl_b32 s68, s50, 5\n" \
  "s_add_u32 s55, s55, s68\n" \
  "s_add_u32 s55, s55, 2 * %[lead]\n" \
  "s_add_u32 s50, s50, 0x10000\n" \
  "s_mov_b32 s48, %[tau]\n" \
  "s_cmp_eq_u32 s75, 1\n" \
  "s_waitcnt vmcnt(0)\n" \
  "v_add_u32 v11, -1, v10\n" \
  "v_min_u32 v11, 0xfff, v11\n" \
    \
  "s_cbranch_scc1 .Ldchunk_%=\n" \
    \
  ".Ldpart_%=:\n" \
  "v_lshrrev_b32 v17, 5, v11\n" \
  "v_add_u32 v17, 1, v17\n" \
  "s_nop 1\n" \
  "v_add_u32_dpp v18, v17, v17 row_shr:1 row_mask:0xf bank_mask:0xf bound_ctrl:1\n" \
  "s_nop 1\n" \
  "v_add_u32_dpp v18, v18, v18 row_shr:2 row_mask:0xf bank_mask:0xf bound_ctrl:1\n" \
  "s_nop 1\n" \
  "v_add_u32_dpp v18, v18, v18 row_shr:4 row_mask:0xf bank_mask:0xf bound_ctrl:1\n" \
  "s_nop 1\n" \
  "v_add_u32_dpp v18, v18, v18 row_shr:8 row_mask:0xf bank_mask:0xf bound_ctrl:1\n" \
  "s_mov_b32 s90, 1\n" \
  "v_readlane_b32 s49, v18, 15\n" \
  "s_add_i32 s87, s49, %[xc]\n" \
  "s_lshr_b32 s87, s87, 5\n" \
  "s_cmp_lt_u32 s71, 16\n" \
  "v_min_i32 v13, s87, v17\n" \
  "s_cbranch_scc1 .Ldpown_%=\n" \
  "v_sub_u32 v19, v17, v13\n" \
  "v_min_u32 v20, 1, v19\n" \
  "v_lshl_add_u32 v20, v19, 1, v20\n" \
  "s_nop 1\n" \
  "v_add_u32_dpp v21, v20, v20 row_shr:1 row_mask:0xf bank_mask:0xf bound_ctrl:1\n" \
  "s_nop 1\n" \
  "v_add_u32_dpp v21, v21, v21 row_shr:2 row_mask:0xf bank_mask:0xf bound_ctrl:1\n" \
  "s_nop 1\n" \
  "v_add_u32_dpp v21, v21, v21 row_shr:4 row_mask:0xf bank_mask:0xf bound_ctrl:1\n" \
  "s_nop 1\n" \
  "v_add_u32_dpp v21, v21, v21 row_shr:8 row_mask:0xf bank_mask:0xf bound_ctrl:1\n" \
  "v_lshlrev_b32 v20, 1, v19\n" \
  "v_readlane_b32 s89, v21, 15\n" \
  "v_sub_u32 v21, v21, v20\n" \
  "s_sub_i32 s89, s89, 1\n" \
  "s_sub_i32 s87, s71, 16\n" \
  "s_mul_i32 s87, s87, s89\n" \
  "s_add_i32 s87, s87, 32\n" \
  "s_add_i32 s88, s87, s89\n" \
  "s_ashr_i32 s87, s87, 4\n" \
  "s_ashr_i32 s88, s88, 4\n" \
  "v_sub_u32 v17, s87, v21\n" \
  "v_sub_u32 v18, s88, v21\n" \
  "v_ashrrev_i32 v17, 1, v17\n" \
  "v_ashrrev_i32 v18, 1, v18\n" \
  "v_med3_i32 v17, v17, 0, v19\n" \
  "v_med3_i32 v18, v18, 0, v19\n" \
  "v_sub_u32 v16, v18, v17\n" \
  "v_add_u32 v13, v13, v17\n" \
  "v_cmp_lt_i32 vcc, 0, v16\n" \
  "s_and_b32 s76, vcc_lo, 0xffff\n" \
    \
  ".Ldnext_%=:\n" \
  "s_cmp_eq_u32 s76, 0\n" \
  "s_cbranch_scc1 .Ldend_%=\n" \
  "s_ff1_i32_b32 s73, s76\n" \
  "s_bitset0_b32 s76, s73\n" \
  "v_readlane_b32 s74, v13, s73\n" \
  "v_readlane_b32 s75, v16, s73\n" \
    \
  ".Ldchunk_%=:\n" \
  "s_sub_i32 s87, s73, s72\n" \
  "v_readlane_b32 s88, v11, s73\n" \
  "v_add_u32 v17, s74, v1\n" \
  "v_min_u32 v17, 0x7f, v17\n" \
  "s_cmp_gt_u32 s87, 2\n" \
  "s_cbranch_scc1 .Ldfar_%=\n" \
  "s_lshl_b32 s87, s87, 10\n" \
  "s_add_i32 s87, s87, %[bt]\n" \
  "v_lshl_add_u32 v18, v17, 3, s87\n" \
  "ds_read_b64 v[14:15], v18\n" \
  ".Ldgot_%=:\n" \
    \
  "s_lshr_b32 s88, s88, 4\n" \
  "v_lshlrev_b32 v17, 1, v17\n" \
  "v_mov_b32 v18, s54\n" \
  "v_cmp_gt_u32 vcc, s88, v17\n" \
  "s_min_i32 s79, s75, 64\n" \
  "s_mov_b32 s78, 0\n" \
  "s_waitcnt lgkmcnt(0)\n" \
  "v_lshlrev_b32 v14, 15, v14\n" \
  "v_lshlrev_b32 v15, 15, v15\n" \
  "v_cndmask_b32 v15, v18, v15, vcc\n" \
    \
    \
  ".Ldtile_%=:\n" \
  "v_readlane_b32 s83, v14, s78\n" \
  "v_readlane_b32 s84, v15, s78\n" \
  "s_sub_i32 s87, s77, 7\n" \
  "s_cmp_lt_i32 s82, s87\n" \
  "s_cbranch_scc1 .Ldwfree_%=\n" \
  ".Ldfree_%=:\n" \
  "s_cmp_eq_u32 s77, s91\n" \
  "s_cbranch_scc1 .Ldevent_%=\n" \
  ".Ldevd_%=:\n" \
  "s_mov_b32 m0, s80\n" \
  "s_add_u32 s85, s83, 0x4000\n" \
  "s_add_u32 s86, s84, 0x4000\n" \
  ".Ldpace0_%=:\n" \
  "s_lshl_b32 s87, s50, 5\n" \
  "s_sub_i32 s87, s87, s55\n" \
  "s_cmp_ge_i32 s87, 0\n" \
  "s_cbranch_scc1 .Ldpaced0_%=\n" \
  "s_memrealtime s[50:51]\n" \
  "s_waitcnt lgkmcnt(0)\n" \
  "s_branch .Ldpace0_%=\n" \
  ".Ldpaced0_%=:\n" \
  "s_add_u32 s55, s55, s48\n" \
  "s_waitcnt vmcnt(22)\n" \
  "buffer_load_dwordx4 v2, s[60:63], s83 offen nt lds\n" \
  "s_add_u32 m0, m0, 0x400\n" \
  "s_waitcnt vmcnt(22)\n" \
  "buffer_load_dwordx4 v3, s[60:63], s83 offen nt lds\n" \
  "s_add_u32 m0, m0, 0x400\n" \
  "s_waitcnt vmcnt(22)\n" \
  "buffer_load_dwordx4 v4, s[60:63], s83 offen nt lds\n" \
  "s_add_u32 m0, m0, 0x400\n" \
  "s_waitcnt vmcnt(22)\n" \
  "buffer_load_dwordx4 v5, s[60:63], s83 offen nt lds\n" \
  "s_add_u32 m0, m0, 0x400\n" \
  "s_waitcnt vmcnt(22)\n" \
  "buffer_load_dwordx4 v2, s[60:63], s84 offen nt lds\n" \
  "s_add_u32 m0, m0, 0x400\n" \
  "s_waitcnt vmcnt(22)\n" \
  "buffer_load_dwordx4 v3, s[60:63], s84 offen nt lds\n" \
  "s_add_u32 m0, m0, 0x400\n" \
  "s_waitcnt vmcnt(22)\n" \
  "ds_max_i32 v7, v12\n" \
  "v_add_u32 v12, 1, v12\n" \
  "buffer_load_dwordx4 v4, s[60:63], s84 offen nt lds\n" \
  "s_add_u32 m0, m0, 0x400\n" \
  "s_waitcnt vmcnt(22)\n" \
  "buffer_load_dwordx4 v5, s[60:63], s84 offen nt lds\n" \
  "s_add_u32 m0, m0, 0x400\n" \
  ".Ldpace1_%=:\n" \
  "s_lshl_b32 s87, s50, 5\n" \
  "s_sub_i32 s87, s87, s55\n" \
  "s_cmp_ge_i32 s87, 0\n" \
  "s_cbranch_scc1 .Ldpaced1_%=\n" \
  "s_memrealtime s[50:51]\n" \
  "s_waitcnt lgkmcnt(0)\n" \
  "s_branch .Ldpace1_%=\n" \
  ".Ldpaced1_%=:\n" \
  "s_add_u32 s55, s55, s48\n" \
  "s_waitcnt vmcnt(22)\n" \
  "buffer_load_dwordx4 v2, s[64:67], s83 offen nt lds\n" \
  "s_add_u32 m0, m0, 0x400\n" \
  "s_waitcnt vmcnt(22)\n" \
  "buffer_load_dwordx4 v6, s[64:67], s83 offen nt lds\n" \
  "s_add_u32 m0, m0, 0x400\n" \
  "s_waitcnt vmcnt(22)\n" \
  "buffer_load_dwordx4 v2, s[64:67], s85 offen nt lds\n" \
  "ds_read_b128 v[24:27], v8\n" \
  "s_memrealtime s[50:51]\n" \
  "s_add_u32 m0, m0, 0x400\n" \
  "s_waitcnt vmcnt(22)\n" \
  "buffer_load_dwordx4 v6, s[64:67], s85 offen nt lds\n" \
  "s_add_u32 m0, m0, 0x400\n" \
  "s_waitcnt vmcnt(22)\n" \
  "buffer_load_dwordx4 v2, s[64:67], s84 offen nt lds\n" \
  "s_waitcnt lgkmcnt(0)\n" \
  "v_min_u32 v24, v24, v25\n" \
  "v_min3_u32 v24, v24, v26, v27\n" \
  "s_add_u32 m0, m0, 0x400\n" \
  "s_waitcnt vmcnt(22)\n" \
  "buffer_load_dwordx4 v6, s[64:67], s84 offen nt lds\n" \
  "s_add_u32 m0, m0, 0x400\n" \
  "s_waitcnt vmcnt(22)\n" \
  "ds_max_i32 v7, v12\n" \
  "v_add_u32 v12, 1, v12\n" \
  "buffer_load_dwordx4 v2, s[64:67], s86 offen nt lds\n" \
  "v_readfirstlane_b32 s82, v24\n" \
  "s_add_u32 m0, m0, 0x400\n" \
  "s_waitcnt vmcnt(22)\n" \
  "buffer_load_dwordx4 v6, s[64:67], s86 offen nt lds\n" \
  "s_add_i32 s80, s80, 0x4000\n" \
  "s_and_b32 s80, s80, 0x1c000\n" \
  "s_add_i32 s77, s77, 1\n" \
  "s_cmp_eq_u32 s77, s90\n" \
  "s_cbranch_scc1 .Ldhtau_%=\n" \
  ".Ldhtaud_%=:\n" \
  "s_add_i32 s78, s78, 1\n" \
  "s_cmp_lt_i32 s78, s79\n" \
  "s_cbranch_scc1 .Ldtile_%=\n" \
  "s_add_i32 s74, s74, s79\n" \
  "s_sub_i32 s75, s75, s79\n" \
  "s_cmp_gt_i32 s75, 0\n" \
  "s_cbranch_scc1 .Ldchunk_%=\n" \
  "s_cmp_eq_u32 s90, 0\n" \
  "s_cbranch_scc1 .Ldpart_%=\n" \
  "s_branch .Ldnext_%=\n" \
    \
  ".Ldpown_%=:\n" \
  "v_readlane_b32 s75, v13, s71\n" \
    \
  "s_mul_i32 s49, s49, %[tau]\n" \
  "s_lshr_b32 s49, s49, 5\n" \
  "v_cvt_f32_i32 v17, s75\n" \
  "v_cvt_f32_i32 v18, s49\n" \
  "v_rcp_f32 v17, v17\n" \
  "s_nop 0\n" \
  "v_mul_f32 v17, v17, v18\n" \
  "v_cvt_i32_f32 v17, v17\n" \
  "s_nop 0\n" \
  "v_readfirstlane_b32 s48, v17\n" \
  "s_min_u32 s48, s48, %[tau] * 9 / 8\n" \
  "s_mov_b32 s92, s75\n" \
  "s_mul_i32 s91, s75, 7\n" \
  "s_lshr_b32 s91, s91, 4\n" \
  "s_mov_b32 s93, 0\n" \
  "s_cmp_lt_u32 s75, 32\n" \
  "s_cselect_b32 s91, -1, s91\n" \
  "s_cmp_lt_u32 s48, %[tau] * 7 / 8\n" \
  "s_cselect_b32 s91, -1, s91\n" \
  "s_sub_i32 s75, s75, 1\n" \
  "s_cmp_gt_i32 s75, 0\n" \
  "s_cbranch_scc1 .Ldchunk_%=\n" \
  "s_branch .Ldend_%=\n" \
    \
  ".Ldhtau_%=:\n" \
  "s_nop 1\n" \
  "v_add_u32_dpp v19, v16, v16 row_shr:1 row_mask:0xf bank_mask:0xf bound_ctrl:1\n" \
  "s_nop 1\n" \
  "v_add_u32_dpp v19, v19, v19 row_shr:2 row_mask:0xf bank_mask:0xf bound_ctrl:1\n" \
  "s_nop 1\n" \
  "v_add_u32_dpp v19, v19, v19 row_shr:4 row_mask:0xf bank_mask:0xf bound_ctrl:1\n" \
  "s_nop 1\n" \
  "v_add_u32_dpp v19, v19, v19 row_shr:8 row_mask:0xf bank_mask:0xf bound_ctrl:1\n" \
  "s_nop 0\n" \
  "v_readlane_b32 s87, v19, 15\n" \
    \
  "s_mul_i32 s49, s49, %[tau]\n" \
  "s_lshr_b32 s49, s49, 5\n" \
  "v_cvt_f32_i32 v17, s87\n" \
  "v_cvt_f32_i32 v18, s49\n" \
  "v_rcp_f32 v17, v17\n" \
  "s_nop 0\n" \
  "v_mul_f32 v17, v17, v18\n" \
  "v_cvt_i32_f32 v17, v17\n" \
  "s_nop 0\n" \
  "v_readfirstlane_b32 s48, v17\n" \
  "s_min_u32 s48, s48, %[tau] * 9 / 8\n" \
  "s_mov_b32 s92, s87\n" \
  "s_mul_i32 s91, s87, 7\n" \
  "s_lshr_b32 s91, s91, 4\n" \
  "s_mov_b32 s93, 0\n" \
  "s_cmp_lt_u32 s87, 32\n" \
  "s_cselect_b32 s91, -1, s91\n" \
  "s_cmp_lt_u32 s48, %[tau] * 7 / 8\n" \
  "s_cselect_b32 s91, -1, s91\n" \
  "s_branch .Ldhtaud_%=\n" \
    \
    \
  ".Ldevent_%=:\n" \
  "s_memrealtime s[50:51]\n" \
  "s_add_i32 s93, s93, 1\n" \
  "s_cmp_eq_u32 s93, 2\n" \
  "s_cbranch_scc1 .Ldev2_%=\n" \
  "s_cmp_eq_u32 s93, 3\n" \
  "s_cbranch_scc1 .Ldev3_%=\n" \
  "s_mul_i32 s91, s92, 9\n" \
  "s_lshr_b32 s91, s91, 4\n" \
  "s_and_b32 s87, s71, 7\n" \
  "s_cmp_eq_u32 s87, 3\n" \
  "s_waitcnt lgkmcnt(0)\n" \
  "s_cbranch_scc0 .Ldevd_%=\n" \
  "s_lshl_b32 s87, s50, 5\n" \
  "s_sub_i32 s87, s87, s55\n" \
  "s_max_i32 s87, s87, 0\n" \
  "v_mov_b32 v24, s50\n" \
  "v_mov_b32 v25, s87\n" \
  "s_lshl_b32 s87, s70, 5\n" \
  "s_and_b32 s88, s71, 24\n" \
  "s_add_i32 s87, s87, s88\n" \
  "v_mov_b32 v17, s87\n" \
  "global_store_dwordx2 v17, v[24:25], s[52:53] sc1\n" \
  "s_branch .Ldevd_%=\n" \
  ".Ldev2_%=:\n" \
  "s_mov_b32 m0, %[junk] + 1024\n" \
  "v_lshlrev_b32 v17, 2, v1\n" \
  "s_add_i32 s91, s91, 2\n" \
  "global_load_lds_dword v17, s[52:53] sc1\n" \
  "s_waitcnt lgkmcnt(0)\n" \
  "s_branch .Ldevd_%=\n" \
  ".Ldev3_%=:\n" \
  "v_and_b32 v17, 31, v1\n" \
  "v_lshlrev_b32 v17, 3, v17\n" \
  "v_add_u32 v17, %[junk] + 1024, v17\n" \
  "ds_read_b64 v[24:25], v17\n" \
  "s_mov_b32 s91, -1\n" \
  "s_waitcnt lgkmcnt(0)\n" \
  "v_sub_u32 v17, s50, v24\n" \
  "v_min_u32 v18, 0x4000, v25\n" \
  "v_cmp_gt_u32 vcc, 1000, v17\n" \
  "s_bcnt1_i32_b32 s88, vcc_lo\n" \
  "s_cmp_lt_u32 s88, 24\n" \
  "v_cndmask_b32 v18, 0, v18, vcc\n" \
  "s_cbranch_scc1 .Ldevd_%=\n" \
  "s_nop 1\n" \
  "v_add_u32_dpp v19, v18, v18 row_shr:1 row_mask:0xf bank_mask:0xf bound_ctrl:1\n" \
  "s_nop 1\n" \
  "v_add_u32_dpp v19, v19, v19 row_shr:2 row_mask:0xf bank_mask:0xf bound_ctrl:1\n" \
  "s_nop 1\n" \
  "v_add_u32_dpp v19, v19, v19 row_shr:4 row_mask:0xf bank_mask:0xf bound_ctrl:1\n" \
  "s_nop 1\n" \
  "v_add_u32_dpp v19, v19, v19 row_shr:8 row_mask:0xf bank_mask:0xf bound_ctrl:1\n" \
  "s_nop 0\n" \
  "v_readlane_b32 s87, v19, 15\n" \
  "v_readlane_b32 s85, v19, 31\n" \
  "s_add_i32 s87, s87, s85\n" \
    \
  "s_mul_i32 s85, s88, %[fl]\n" \
  "s_cmp_lt_u32 s87, s85\n" \
  "s_cbranch_scc1 .Ldevd_%=\n" \
  "s_lshl_b32 s87, s87, 4\n" \
  "s_mul_i32 s85, s88, 7 * %[fa]\n" \
  "s_add_i32 s87, s87, s85\n" \
  "s_mul_i32 s88, s88, s92\n" \
  "s_mul_i32 s88, s88, 14\n" \
  "v_cvt_f32_i32 v17, s88\n" \
  "v_cvt_f32_i32 v18, s87\n" \
  "v_rcp_f32 v17, v17\n" \
  "s_nop 0\n" \
  "v_mul_f32 v17, v17, v18\n" \
  "v_cvt_i32_f32 v17, v17\n" \
  "s_nop 0\n" \
  "v_readfirstlane_b32 s87, v17\n" \
  "s_max_i32 s87, s87, 0\n" \
  "s_add_i32 s48, s48, s87\n" \
  "s_mul_i32 s87, s87, s77\n" \
  "s_lshl_b32 s87, s87, 1\n" \
  "s_add_u32 s55, s55, s87\n" \
  "s_branch .Ldevd_%=\n" \
    \
  ".Ldfar_%=:\n" \
  "s_lshl_b32 s87, s73, 10\n" \
  "v_lshlrev_b32 v18, 3, v17\n" \
  "buffer_load_dwordx2 v[14:15], v18, s[56:59], s87 offen\n" \
  "s_waitcnt vmcnt(0)\n" \
  "s_branch .Ldgot_%=\n" \
    \
  ".Ldwfree_%=:\n" \
  "ds_read_b128 v[24:27], v8\n" \
  "s_addk_i32 s81, -1\n" \
  "s_waitcnt lgkmcnt(0)\n" \
  "v_min_u32 v24, v24, v25\n" \
  "v_min3_u32 v24, v24, v26, v27\n" \
  "s_cmp_lt_i32 s81, 0\n" \
  "s_cbranch_scc1 .Ldfree_%=\n" \
  "v_readfirstlane_b32 s82, v24\n" \
  "s_cmp_lt_i32 s82, s87\n" \
  "s_cbranch_scc1 .Ldwfree_%=\n" \
  "s_branch .Ldfree_%=\n" \
    \
  ".Ldend_%=:\n" \
  "s_lshl_b32 s87, s77, 1\n" \
  "s_sub_i32 s87, s87, 2\n" \
  "v_mov_b32 v12, s87\n" \
  "s_waitcnt vmcnt(16)\n" \
  "ds_max_i32 v7, v12\n" \
  "v_add_u32 v12, 1, v12\n" \
  "s_waitcnt vmcnt(8)\n" \
  "ds_max_i32 v7, v12\n" \
  "v_add_u32 v12, 1, v12\n" \
  "s_waitcnt vmcnt(0)\n" \
  "ds_max_i32 v7, v12\n" \
  "v_add_u32 v12, 1, v12\n" \
  "s_endpgm\n" \
  ".Ldskip_%=:\n"
#define LOADER_CLOB "v1", "s40", "s41", "s42", "s43", "s44", "s45", "s46", "s47", "s48", "s49", "s50", "s51", "s52", "s53", "s54", "s55", "s56", "s57", "s58", "s59", "s60", "s61", "s62", "s63", "s64", "s65", "s66", "s67", "s68", "s69", "s70", "s71", "s72", "s73", "s74", "s75", "s76", "s77", "s78", "s79", "s80", "s81", "s82", "s83", "s84", "s85", "s86", "s87", "s88", "s89", "s90", "s91", "s92", "s93"

#define TILE_LOOP_ASM \
  "s_waitcnt lgkmcnt(0)\n" \
  "v_mov_b32 v200, 0x3f803f80\n" \
  "v_mov_b32 v201, 0x3f803f80\n" \
  "v_mov_b32 v202, 0x3f803f80\n" \
  "v_mov_b32 v203, 0x3f803f80\n" \
  "s_lshl_b32 %[nv], %[tg], 1\n" \
  "s_add_i32 %[nv], %[nv], 2\n" \
  "s_cmp_eq_u32 %[kr], 0\n" \
  "s_cbranch_scc1 .LpollK_%=\n" \
    \
  ".Ltop_%=:\n" \
  "s_cmp_lt_i32 %[full], %[nv]\n" \
  "s_cbranch_scc1 .LqkL_%=\n" \
  "s_waitcnt lgkmcnt(0)\n" \
  "v_add_u32 v206, %[slot], %[rdv]\n" \
  "v_mfma_f32_16x16x32_bf16 v[180:183], v[116:119], v[100:103], 0\n" \
  "v_xor_b32 v207, 32, v206\n" \
  "ds_read_b64_tr_b16 v[148:149], v206 offset:0\n" \
  "ds_read_b64_tr_b16 v[150:151], v206 offset:4096\n" \
  "v_mfma_f32_16x16x32_bf16 v[180:183], v[120:123], v[104:107], v[180:183]\n" \
  "ds_read_b64_tr_b16 v[152:153], v207 offset:0\n" \
  "ds_read_b64_tr_b16 v[154:155], v207 offset:4096\n" \
  "ds_read_b64_tr_b16 v[156:157], v206 offset:256\n" \
  "v_mfma_f32_16x16x32_bf16 v[180:183], v[124:127], v[108:111], v[180:183]\n" \
  "ds_read_b64_tr_b16 v[158:159], v206 offset:4352\n" \
  "ds_read_b64_tr_b16 v[160:161], v207 offset:256\n" \
  "ds_read_b64_tr_b16 v[162:163], v207 offset:4352\n" \
  "v_mfma_f32_16x16x32_bf16 v[180:183], v[128:131], v[112:115], v[180:183]\n" \
  "ds_read_b64_tr_b16 v[164:165], v206 offset:512\n" \
  "ds_read_b64_tr_b16 v[166:167], v206 offset:4608\n" \
  "ds_read_b64_tr_b16 v[168:169], v207 offset:512\n" \
  "v_mfma_f32_16x16x32_bf16 v[184:187], v[132:135], v[100:103], 0\n" \
  "ds_read_b64_tr_b16 v[170:171], v207 offset:4608\n" \
  "ds_read_b64_tr_b16 v[172:173], v206 offset:768\n" \
  "ds_read_b64_tr_b16 v[174:175], v206 offset:4864\n" \
  "v_mfma_f32_16x16x32_bf16 v[184:187], v[136:139], v[104:107], v[184:187]\n" \
  "ds_read_b64_tr_b16 v[176:177], v207 offset:768\n" \
  "ds_read_b64_tr_b16 v[178:179], v207 offset:4864\n" \
  "s_add_i32 %[tmp], %[tg], 1\n" \
  "v_mfma_f32_16x16x32_bf16 v[184:187], v[140:143], v[108:111], v[184:187]\n" \
  "v_mov_b32 v210, %[tmp]\n" \
  "s_mov_b32 %[vd], 1\n" \
  "ds_write_b32 %[done], v210\n" \
  "v_mfma_f32_16x16x32_bf16 v[184:187], v[144:147], v[112:115], v[184:187]\n" \
    \
  ".Lsm_%=:\n" \
  "s_cmp_ge_i32 %[tg], %[maskat]\n" \
  "s_cbranch_scc1 .Lmask_%=\n" \
  ".Lsm2_%=:\n" \
  "v_fma_f32 v188, v180, %[c], %[nm]\n" \
  "v_fma_f32 v189, v181, %[c], %[nm]\n" \
  "v_fma_f32 v190, v182, %[c], %[nm]\n" \
  "v_fma_f32 v191, v183, %[c], %[nm]\n" \
  "v_maximum3_f32 v208, v180, v181, v182\n" \
  "v_exp_f32 v188, v188\n" \
  "v_exp_f32 v189, v189\n" \
  "v_exp_f32 v190, v190\n" \
  "v_exp_f32 v191, v191\n" \
  "v_fma_f32 v192, v184, %[c], %[nm]\n" \
  "v_fma_f32 v193, v185, %[c], %[nm]\n" \
  "v_fma_f32 v194, v186, %[c], %[nm]\n" \
  "v_fma_f32 v195, v187, %[c], %[nm]\n" \
  "v_maximum3_f32 v208, v208, v183, v184\n" \
  "v_maximum3_f32 v209, v185, v186, v187\n" \
  "v_exp_f32 v192, v192\n" \
  "v_exp_f32 v193, v193\n" \
  "v_exp_f32 v194, v194\n" \
  "v_exp_f32 v195, v195\n" \
  "v_maximum3_f32 v208, v208, v209, v209\n" \
  "v_cvt_pk_bf16_f32 v196, v188, v189\n" \
  "v_cvt_pk_bf16_f32 v197, v190, v191\n" \
  "v_cmp_gt_f32 vcc, v208, %[thr]\n" \
  "v_cvt_pk_bf16_f32 v198, v192, v193\n" \
  "v_cvt_pk_bf16_f32 v199, v194, v195\n" \
  "s_cbranch_vccnz .Lresc_%=\n" \
    \
  ".Lpv_%=:\n" \
  "s_cmp_eq_u32 %[vd], 0\n" \
  "s_cbranch_scc1 .LvL_%=\n" \
  ".Lpv2_%=:\n" \
  "v_mfma_f32_16x16x32_bf16 v[96:99], v[200:203], v[196:199], v[96:99]\n" \
  "s_add_i32 %[tg], %[tg], 1\n" \
  "s_add_i32 %[slot], %[slot], 0x4000\n" \
  "s_and_b32 %[slot], %[slot], 0x1c000\n" \
  "s_cmp_le_i32 %[full], %[nv]\n" \
  "s_cbranch_scc1 .LpvL_%=\n" \
  "v_add_u32 v205, %[slot], %[rdk]\n" \
  "s_add_i32 %[nv], %[nv], 2\n" \
  "s_waitcnt lgkmcnt(0)\n" \
  "v_mfma_f32_16x16x32_bf16 v[64:67], v[148:151], v[196:199], v[64:67]\n" \
  "ds_read_b128 v[116:119], v205 offset:0\n" \
  "ds_read_b128 v[120:123], v205 offset:256\n" \
  "v_mfma_f32_16x16x32_bf16 v[68:71], v[152:155], v[196:199], v[68:71]\n" \
  "ds_read_b128 v[124:127], v205 offset:512\n" \
  "ds_read_b128 v[128:131], v205 offset:768\n" \
  "v_mfma_f32_16x16x32_bf16 v[72:75], v[156:159], v[196:199], v[72:75]\n" \
  "ds_read_b128 v[132:135], v205 offset:4096\n" \
  "ds_read_b128 v[136:139], v205 offset:4352\n" \
  "v_mfma_f32_16x16x32_bf16 v[76:79], v[160:163], v[196:199], v[76:79]\n" \
  "ds_read_b128 v[140:143], v205 offset:4608\n" \
  "ds_read_b128 v[144:147], v205 offset:4864\n" \
  "v_mfma_f32_16x16x32_bf16 v[80:83], v[164:167], v[196:199], v[80:83]\n" \
  "ds_read_b32 v204, %[ctl]\n" \
  "v_mfma_f32_16x16x32_bf16 v[84:87], v[168:171], v[196:199], v[84:87]\n" \
  "v_mfma_f32_16x16x32_bf16 v[88:91], v[172:175], v[196:199], v[88:91]\n" \
  "v_mfma_f32_16x16x32_bf16 v[92:95], v[176:179], v[196:199], v[92:95]\n" \
  "s_waitcnt lgkmcnt(0)\n" \
  "v_readfirstlane_b32 %[full], v204\n" \
  "s_cmp_lt_i32 %[tg], %[stop]\n" \
  "s_cbranch_scc1 .Ltop_%=\n" \
  "s_mov_b32 %[kr], 1\n" \
  "s_branch .Lexit_%=\n" \
    \
  ".Lmask_%=:\n" \
  "s_lshl_b32 %[tmp], %[tg], 5\n" \
  "s_sub_i32 %[tmp], %[limb], %[tmp]\n" \
  "v_sub_u32 v208, %[tmp], %[g4]\n" \
  "v_mov_b32 v209, 0xff800000\n" \
  "s_nop 7\n" \
  "s_cmp_gt_i32 %[zq], 0\n" \
  "s_cbranch_scc1 .Lmask2_%=\n" \
  "v_mov_b32 v180, 0\n" \
  "v_mov_b32 v181, 0\n" \
  "v_mov_b32 v182, 0\n" \
  "v_mov_b32 v183, 0\n" \
  "v_mov_b32 v184, 0\n" \
  "v_mov_b32 v185, 0\n" \
  "v_mov_b32 v186, 0\n" \
  "v_mov_b32 v187, 0\n" \
  ".Lmask2_%=:\n" \
  "v_cmp_lt_i32 vcc, 0, v208\n" \
  "s_nop 1\n" \
  "v_cndmask_b32 v180, v209, v180, vcc\n" \
  "v_cmp_lt_i32 vcc, 1, v208\n" \
  "s_nop 1\n" \
  "v_cndmask_b32 v181, v209, v181, vcc\n" \
  "v_cmp_lt_i32 vcc, 2, v208\n" \
  "s_nop 1\n" \
  "v_cndmask_b32 v182, v209, v182, vcc\n" \
  "v_cmp_lt_i32 vcc, 3, v208\n" \
  "s_nop 1\n" \
  "v_cndmask_b32 v183, v209, v183, vcc\n" \
  "v_cmp_lt_i32 vcc, 16, v208\n" \
  "s_nop 1\n" \
  "v_cndmask_b32 v184, v209, v184, vcc\n" \
  "v_cmp_lt_i32 vcc, 17, v208\n" \
  "s_nop 1\n" \
  "v_cndmask_b32 v185, v209, v185, vcc\n" \
  "v_cmp_lt_i32 vcc, 18, v208\n" \
  "s_nop 1\n" \
  "v_cndmask_b32 v186, v209, v186, vcc\n" \
  "v_cmp_lt_i32 vcc, 19, v208\n" \
  "s_nop 1\n" \
  "v_cndmask_b32 v187, v209, v187, vcc\n" \
  "s_branch .Lsm2_%=\n" \
    \
  ".Lresc_%=:\n" \
  "v_mov_b32 v209, v208\n" \
  "s_nop 1\n" \
  "v_permlane16_swap_b32 v208, v209\n" \
  "s_nop 1\n" \
  "v_maximum3_f32 v208, v208, v209, v209\n" \
  "v_mov_b32 v209, v208\n" \
  "s_nop 1\n" \
  "v_permlane32_swap_b32 v208, v209\n" \
  "s_nop 1\n" \
  "v_maximum3_f32 v208, v208, v209, v209\n" \
  "v_mul_f32 v208, %[c], v208\n" \
  "v_ceil_f32 v208, v208\n" \
  "v_maximum3_f32 v208, v208, -%[nm], -%[nm]\n" \
  "v_add_f32 v209, %[nm], v208\n" \
  "v_exp_f32 v209, -v209\n" \
  "v_xor_b32 %[nm], 0x80000000, v208\n" \
  "v_mul_f32 %[thr], %[ic], v208\n" \
  "v_mul_f32 v64, v209, v64\n" \
  "v_mul_f32 v65, v209, v65\n" \
  "v_mul_f32 v66, v209, v66\n" \
  "v_mul_f32 v67, v209, v67\n" \
  "v_mul_f32 v68, v209, v68\n" \
  "v_mul_f32 v69, v209, v69\n" \
  "v_mul_f32 v70, v209, v70\n" \
  "v_mul_f32 v71, v209, v71\n" \
  "v_mul_f32 v72, v209, v72\n" \
  "v_mul_f32 v73, v209, v73\n" \
  "v_mul_f32 v74, v209, v74\n" \
  "v_mul_f32 v75, v209, v75\n" \
  "v_mul_f32 v76, v209, v76\n" \
  "v_mul_f32 v77, v209, v77\n" \
  "v_mul_f32 v78, v209, v78\n" \
  "v_mul_f32 v79, v209, v79\n" \
  "v_mul_f32 v80, v209, v80\n" \
  "v_mul_f32 v81, v209, v81\n" \
  "v_mul_f32 v82, v209, v82\n" \
  "v_mul_f32 v83, v209, v83\n" \
  "v_mul_f32 v84, v209, v84\n" \
  "v_mul_f32 v85, v209, v85\n" \
  "v_mul_f32 v86, v209, v86\n" \
  "v_mul_f32 v87, v209, v87\n" \
  "v_mul_f32 v88, v209, v88\n" \
  "v_mul_f32 v89, v209, v89\n" \
  "v_mul_f32 v90, v209, v90\n" \
  "v_mul_f32 v91, v209, v91\n" \
  "v_mul_f32 v92, v209, v92\n" \
  "v_mul_f32 v93, v209, v93\n" \
  "v_mul_f32 v94, v209, v94\n" \
  "v_mul_f32 v95, v209, v95\n" \
  "v_mul_f32 v96, v209, v96\n" \
  "v_mul_f32 v97, v209, v97\n" \
  "v_mul_f32 v98, v209, v98\n" \
  "v_mul_f32 v99, v209, v99\n" \
  "v_fma_f32 v188, v180, %[c], %[nm]\n" \
  "v_fma_f32 v189, v181, %[c], %[nm]\n" \
  "v_fma_f32 v190, v182, %[c], %[nm]\n" \
  "v_fma_f32 v191, v183, %[c], %[nm]\n" \
  "v_fma_f32 v192, v184, %[c], %[nm]\n" \
  "v_fma_f32 v193, v185, %[c], %[nm]\n" \
  "v_fma_f32 v194, v186, %[c], %[nm]\n" \
  "v_fma_f32 v195, v187, %[c], %[nm]\n" \
  "v_exp_f32 v188, v188\n" \
  "v_exp_f32 v189, v189\n" \
  "v_exp_f32 v190, v190\n" \
  "v_exp_f32 v191, v191\n" \
  "v_exp_f32 v192, v192\n" \
  "v_exp_f32 v193, v193\n" \
  "v_exp_f32 v194, v194\n" \
  "v_exp_f32 v195, v195\n" \
  "s_nop 0\n" \
  "v_cvt_pk_bf16_f32 v196, v188, v189\n" \
  "v_cvt_pk_bf16_f32 v197, v190, v191\n" \
  "v_cvt_pk_bf16_f32 v198, v192, v193\n" \
  "v_cvt_pk_bf16_f32 v199, v194, v195\n" \
  "s_branch .Lpv_%=\n" \
    \
  ".LpollK_%=:\n" \
  "v_add_u32 v205, %[slot], %[rdk]\n" \
  "s_add_i32 %[tmp], %[nv], -1\n" \
  ".LpK_%=:\n" \
  "ds_read_b32 v204, %[ctl]\n" \
  "s_addk_i32 %[spin], -1\n" \
  "s_cmp_lt_i32 %[spin], 0\n" \
  "s_cbranch_scc1 .LpKx_%=\n" \
  "s_waitcnt lgkmcnt(0)\n" \
  "v_readfirstlane_b32 %[full], v204\n" \
  "s_cmp_lt_i32 %[full], %[tmp]\n" \
  "s_cbranch_scc1 .LpK_%=\n" \
  ".LpKx_%=:\n" \
  "ds_read_b128 v[116:119], v205 offset:0\n" \
  "ds_read_b128 v[120:123], v205 offset:256\n" \
  "ds_read_b128 v[124:127], v205 offset:512\n" \
  "ds_read_b128 v[128:131], v205 offset:768\n" \
  "ds_read_b128 v[132:135], v205 offset:4096\n" \
  "ds_read_b128 v[136:139], v205 offset:4352\n" \
  "ds_read_b128 v[140:143], v205 offset:4608\n" \
  "ds_read_b128 v[144:147], v205 offset:4864\n" \
  "s_cmp_ge_i32 %[full], %[nv]\n" \
  "s_cbranch_scc1 .Ltop_%=\n" \
  ".LqkL_%=:\n" \
  "s_waitcnt lgkmcnt(7)\n" \
  "v_mfma_f32_16x16x32_bf16 v[180:183], v[116:119], v[100:103], 0\n" \
  "s_waitcnt lgkmcnt(6)\n" \
  "v_mfma_f32_16x16x32_bf16 v[180:183], v[120:123], v[104:107], v[180:183]\n" \
  "s_waitcnt lgkmcnt(5)\n" \
  "v_mfma_f32_16x16x32_bf16 v[180:183], v[124:127], v[108:111], v[180:183]\n" \
  "s_waitcnt lgkmcnt(4)\n" \
  "v_mfma_f32_16x16x32_bf16 v[180:183], v[128:131], v[112:115], v[180:183]\n" \
  "s_waitcnt lgkmcnt(3)\n" \
  "v_mfma_f32_16x16x32_bf16 v[184:187], v[132:135], v[100:103], 0\n" \
  "s_waitcnt lgkmcnt(2)\n" \
  "v_mfma_f32_16x16x32_bf16 v[184:187], v[136:139], v[104:107], v[184:187]\n" \
  "s_waitcnt lgkmcnt(1)\n" \
  "v_mfma_f32_16x16x32_bf16 v[184:187], v[140:143], v[108:111], v[184:187]\n" \
  "s_waitcnt lgkmcnt(0)\n" \
  "v_mfma_f32_16x16x32_bf16 v[184:187], v[144:147], v[112:115], v[184:187]\n" \
  "s_mov_b32 %[vd], 0\n" \
  "s_branch .Lsm_%=\n" \
    \
  ".LvL_%=:\n" \
  "v_add_u32 v206, %[slot], %[rdv]\n" \
  "s_add_i32 %[tmp], %[tg], 1\n" \
  "v_xor_b32 v207, 32, v206\n" \
  "v_mov_b32 v210, %[tmp]\n" \
  ".LpV_%=:\n" \
  "ds_read_b32 v204, %[ctl]\n" \
  "s_addk_i32 %[spin], -1\n" \
  "s_cmp_lt_i32 %[spin], 0\n" \
  "s_cbranch_scc1 .LpVx_%=\n" \
  "s_waitcnt lgkmcnt(0)\n" \
  "v_readfirstlane_b32 %[full], v204\n" \
  "s_cmp_lt_i32 %[full], %[nv]\n" \
  "s_cbranch_scc1 .LpV_%=\n" \
  ".LpVx_%=:\n" \
  "ds_read_b64_tr_b16 v[148:149], v206 offset:0\n" \
  "ds_read_b64_tr_b16 v[150:151], v206 offset:4096\n" \
  "ds_read_b64_tr_b16 v[152:153], v207 offset:0\n" \
  "ds_read_b64_tr_b16 v[154:155], v207 offset:4096\n" \
  "ds_read_b64_tr_b16 v[156:157], v206 offset:256\n" \
  "ds_read_b64_tr_b16 v[158:159], v206 offset:4352\n" \
  "ds_read_b64_tr_b16 v[160:161], v207 offset:256\n" \
  "ds_read_b64_tr_b16 v[162:163], v207 offset:4352\n" \
  "ds_read_b64_tr_b16 v[164:165], v206 offset:512\n" \
  "ds_read_b64_tr_b16 v[166:167], v206 offset:4608\n" \
  "ds_read_b64_tr_b16 v[168:169], v207 offset:512\n" \
  "ds_read_b64_tr_b16 v[170:171], v207 offset:4608\n" \
  "ds_read_b64_tr_b16 v[172:173], v206 offset:768\n" \
  "ds_read_b64_tr_b16 v[174:175], v206 offset:4864\n" \
  "ds_read_b64_tr_b16 v[176:177], v207 offset:768\n" \
  "ds_read_b64_tr_b16 v[178:179], v207 offset:4864\n" \
  "ds_write_b32 %[done], v210\n" \
  "s_branch .Lpv2_%=\n" \
    \
  ".LpvL_%=:\n" \
  "s_add_i32 %[nv], %[nv], 2\n" \
  "s_waitcnt lgkmcnt(15)\n" \
  "v_mfma_f32_16x16x32_bf16 v[64:67], v[148:151], v[196:199], v[64:67]\n" \
  "s_waitcnt lgkmcnt(13)\n" \
  "v_mfma_f32_16x16x32_bf16 v[68:71], v[152:155], v[196:199], v[68:71]\n" \
  "s_waitcnt lgkmcnt(11)\n" \
  "v_mfma_f32_16x16x32_bf16 v[72:75], v[156:159], v[196:199], v[72:75]\n" \
  "s_waitcnt lgkmcnt(9)\n" \
  "v_mfma_f32_16x16x32_bf16 v[76:79], v[160:163], v[196:199], v[76:79]\n" \
  "s_waitcnt lgkmcnt(7)\n" \
  "v_mfma_f32_16x16x32_bf16 v[80:83], v[164:167], v[196:199], v[80:83]\n" \
  "s_waitcnt lgkmcnt(5)\n" \
  "v_mfma_f32_16x16x32_bf16 v[84:87], v[168:171], v[196:199], v[84:87]\n" \
  "s_waitcnt lgkmcnt(3)\n" \
  "v_mfma_f32_16x16x32_bf16 v[88:91], v[172:175], v[196:199], v[88:91]\n" \
  "s_waitcnt lgkmcnt(1)\n" \
  "v_mfma_f32_16x16x32_bf16 v[92:95], v[176:179], v[196:199], v[92:95]\n" \
  "s_mov_b32 %[kr], 0\n" \
  "s_cmp_lt_i32 %[tg], %[stop]\n" \
  "s_cbranch_scc1 .LpollK_%=\n" \
  ".Lexit_%=:\n" \
  "s_nop 7\n"
#define TILE_CLOB "v148", "v149", "v150", "v151", "v152", "v153", "v154", "v155", "v156", "v157", "v158", "v159", "v160", "v161", "v162", "v163", "v164", "v165", "v166", "v167", "v168", "v169", "v170", "v171", "v172", "v173", "v174", "v175", "v176", "v177", "v178", "v179", "v180", "v181", "v182", "v183", "v184", "v185", "v186", "v187", "v188", "v189", "v190", "v191", "v192", "v193", "v194", "v195", "v196", "v197", "v198", "v199", "v200", "v201", "v202", "v203", "v204", "v205", "v206", "v207", "v208", "v209", "v210"

#define FINISH_ASM \
  "s_cmp_lt_i32 %[k], 1\n" \
  "s_cbranch_scc1 .Lfout_%=\n" \
  "s_bfm_b32 %[need], %[k], 0\n" \
  "v_mov_b32 v190, %[thi]\n" \
    \
  ".Lfp_%=:\n" \
  "buffer_load_dwordx4 v[200:203], %[fo], %[sp], 0 offen offset:128 sc1\n" \
  "s_addk_i32 %[spin], -1\n" \
  "s_waitcnt vmcnt(0)\n" \
  "v_add_co_u32 v188, vcc, %[tlo], v202\n" \
  "s_cmp_lt_i32 %[spin], 0\n" \
  "s_cbranch_scc1 .Lfpx_%=\n" \
  "v_addc_co_u32 v189, vcc, v190, v203, vcc\n" \
  "v_cmp_eq_u64 vcc, v[188:189], v[200:201]\n" \
  "s_and_b32 %[tmp], vcc_lo, %[need]\n" \
  "s_cmp_lg_u32 %[tmp], %[need]\n" \
  "s_cbranch_scc1 .Lfp_%=\n" \
  ".Lfpx_%=:\n" \
  "s_mov_b32 %[c], 0\n" \
    \
  ".Lfbatch_%=:\n" \
  "v_readlane_b32 %[tmp], %[fo], %[c]\n" \
  "s_add_i32 m0, %[slot], 0\n" \
  "s_add_i32 %[c], %[c], 1\n" \
  "s_add_i32 %[t2], %[tmp], -4096\n" \
  "s_cmp_ge_i32 %[c], %[k]\n" \
  "buffer_load_dwordx4 %[l16], %[sp], %[t2] offen offset:0 sc1 lds\n" \
  "buffer_load_dwordx4 %[l16], %[sp], %[t2] offen offset:1024 sc1 lds\n" \
  "buffer_load_dwordx4 %[l16], %[sp], %[t2] offen offset:2048 sc1 lds\n" \
  "buffer_load_dwordx4 %[l16], %[sp], %[t2] offen offset:3072 sc1 lds\n" \
  "buffer_load_dwordx2 v[180:181], %[i8], %[sp], %[tmp] offen sc1\n" \
  "s_cbranch_scc1 .Lfn1_%=\n" \
  "v_readlane_b32 %[tmp], %[fo], %[c]\n" \
  "s_add_i32 m0, %[slot], 4096\n" \
  "s_add_i32 %[c], %[c], 1\n" \
  "s_add_i32 %[t2], %[tmp], -4096\n" \
  "s_cmp_ge_i32 %[c], %[k]\n" \
  "buffer_load_dwordx4 %[l16], %[sp], %[t2] offen offset:0 sc1 lds\n" \
  "buffer_load_dwordx4 %[l16], %[sp], %[t2] offen offset:1024 sc1 lds\n" \
  "buffer_load_dwordx4 %[l16], %[sp], %[t2] offen offset:2048 sc1 lds\n" \
  "buffer_load_dwordx4 %[l16], %[sp], %[t2] offen offset:3072 sc1 lds\n" \
  "buffer_load_dwordx2 v[182:183], %[i8], %[sp], %[tmp] offen sc1\n" \
  "s_cbranch_scc1 .Lfn2_%=\n" \
  "v_readlane_b32 %[tmp], %[fo], %[c]\n" \
  "s_add_i32 m0, %[slot], 8192\n" \
  "s_add_i32 %[c], %[c], 1\n" \
  "s_add_i32 %[t2], %[tmp], -4096\n" \
  "s_cmp_ge_i32 %[c], %[k]\n" \
  "buffer_load_dwordx4 %[l16], %[sp], %[t2] offen offset:0 sc1 lds\n" \
  "buffer_load_dwordx4 %[l16], %[sp], %[t2] offen offset:1024 sc1 lds\n" \
  "buffer_load_dwordx4 %[l16], %[sp], %[t2] offen offset:2048 sc1 lds\n" \
  "buffer_load_dwordx4 %[l16], %[sp], %[t2] offen offset:3072 sc1 lds\n" \
  "buffer_load_dwordx2 v[184:185], %[i8], %[sp], %[tmp] offen sc1\n" \
  "s_cbranch_scc1 .Lfn3_%=\n" \
  "v_readlane_b32 %[tmp], %[fo], %[c]\n" \
  "s_add_i32 m0, %[slot], 12288\n" \
  "s_add_i32 %[c], %[c], 1\n" \
  "s_add_i32 %[t2], %[tmp], -4096\n" \
  "s_cmp_ge_i32 %[c], %[k]\n" \
  "buffer_load_dwordx4 %[l16], %[sp], %[t2] offen offset:0 sc1 lds\n" \
  "buffer_load_dwordx4 %[l16], %[sp], %[t2] offen offset:1024 sc1 lds\n" \
  "buffer_load_dwordx4 %[l16], %[sp], %[t2] offen offset:2048 sc1 lds\n" \
  "buffer_load_dwordx4 %[l16], %[sp], %[t2] offen offset:3072 sc1 lds\n" \
  "buffer_load_dwordx2 v[186:187], %[i8], %[sp], %[tmp] offen sc1\n" \
  "v_add_u32 v192, %[slot], %[rdp]\n" \
  "v_xor_b32 v193, 32, v192\n" \
  "s_mov_b32 %[st], 2\n" \
  "s_waitcnt vmcnt(10)\n" \
  "ds_read_b64_tr_b16 v[148:149], v192 offset:0\n" \
  "ds_read_b64_tr_b16 v[150:151], v192 offset:4096\n" \
  "ds_read_b64_tr_b16 v[152:153], v193 offset:0\n" \
  "ds_read_b64_tr_b16 v[154:155], v193 offset:4096\n" \
  "ds_read_b64_tr_b16 v[156:157], v192 offset:256\n" \
  "ds_read_b64_tr_b16 v[158:159], v192 offset:4352\n" \
  "ds_read_b64_tr_b16 v[160:161], v193 offset:256\n" \
  "ds_read_b64_tr_b16 v[162:163], v193 offset:4352\n" \
  "ds_read_b64_tr_b16 v[164:165], v192 offset:512\n" \
  "ds_read_b64_tr_b16 v[166:167], v192 offset:4608\n" \
  "ds_read_b64_tr_b16 v[168:169], v193 offset:512\n" \
  "ds_read_b64_tr_b16 v[170:171], v193 offset:4608\n" \
  "ds_read_b64_tr_b16 v[172:173], v192 offset:768\n" \
  "ds_read_b64_tr_b16 v[174:175], v192 offset:4864\n" \
  "ds_read_b64_tr_b16 v[176:177], v193 offset:768\n" \
  "ds_read_b64_tr_b16 v[178:179], v193 offset:4864\n" \
  "v_mov_b32 v194, v180\n" \
  "v_mov_b32 v195, v182\n" \
  "v_mov_b32 v204, v181\n" \
  "v_mov_b32 v205, v183\n" \
  "s_mov_b32 %[one], 1.0\n" \
  "s_branch .Lfmath_%=\n" \
  ".Lfn3_%=:\n" \
  "v_add_u32 v192, %[slot], %[rdp]\n" \
  "v_xor_b32 v193, 32, v192\n" \
  "s_mov_b32 %[st], 1\n" \
  "s_waitcnt vmcnt(5)\n" \
  "ds_read_b64_tr_b16 v[148:149], v192 offset:0\n" \
  "ds_read_b64_tr_b16 v[150:151], v192 offset:4096\n" \
  "ds_read_b64_tr_b16 v[152:153], v193 offset:0\n" \
  "ds_read_b64_tr_b16 v[154:155], v193 offset:4096\n" \
  "ds_read_b64_tr_b16 v[156:157], v192 offset:256\n" \
  "ds_read_b64_tr_b16 v[158:159], v192 offset:4352\n" \
  "ds_read_b64_tr_b16 v[160:161], v193 offset:256\n" \
  "ds_read_b64_tr_b16 v[162:163], v193 offset:4352\n" \
  "ds_read_b64_tr_b16 v[164:165], v192 offset:512\n" \
  "ds_read_b64_tr_b16 v[166:167], v192 offset:4608\n" \
  "ds_read_b64_tr_b16 v[168:169], v193 offset:512\n" \
  "ds_read_b64_tr_b16 v[170:171], v193 offset:4608\n" \
  "ds_read_b64_tr_b16 v[172:173], v192 offset:768\n" \
  "ds_read_b64_tr_b16 v[174:175], v192 offset:4864\n" \
  "ds_read_b64_tr_b16 v[176:177], v193 offset:768\n" \
  "ds_read_b64_tr_b16 v[178:179], v193 offset:4864\n" \
  "v_mov_b32 v194, v180\n" \
  "v_mov_b32 v195, v182\n" \
  "v_mov_b32 v204, v181\n" \
  "v_mov_b32 v205, v183\n" \
  "s_mov_b32 %[one], 1.0\n" \
  "s_branch .Lfmath_%=\n" \
  ".Lfn1_%=:\n" \
  "v_add_u32 v192, %[slot], %[rdp]\n" \
  "v_xor_b32 v193, 32, v192\n" \
  "s_mov_b32 %[st], 0\n" \
  "s_waitcnt vmcnt(0)\n" \
  "ds_read_b64_tr_b16 v[148:149], v192 offset:0\n" \
  "ds_read_b64_tr_b16 v[150:151], v192 offset:0\n" \
  "ds_read_b64_tr_b16 v[152:153], v193 offset:0\n" \
  "ds_read_b64_tr_b16 v[154:155], v193 offset:0\n" \
  "ds_read_b64_tr_b16 v[156:157], v192 offset:256\n" \
  "ds_read_b64_tr_b16 v[158:159], v192 offset:256\n" \
  "ds_read_b64_tr_b16 v[160:161], v193 offset:256\n" \
  "ds_read_b64_tr_b16 v[162:163], v193 offset:256\n" \
  "ds_read_b64_tr_b16 v[164:165], v192 offset:512\n" \
  "ds_read_b64_tr_b16 v[166:167], v192 offset:512\n" \
  "ds_read_b64_tr_b16 v[168:169], v193 offset:512\n" \
  "ds_read_b64_tr_b16 v[170:171], v193 offset:512\n" \
  "ds_read_b64_tr_b16 v[172:173], v192 offset:768\n" \
  "ds_read_b64_tr_b16 v[174:175], v192 offset:768\n" \
  "ds_read_b64_tr_b16 v[176:177], v193 offset:768\n" \
  "ds_read_b64_tr_b16 v[178:179], v193 offset:768\n" \
  "v_mov_b32 v194, v180\n" \
  "v_mov_b32 v195, v180\n" \
  "v_mov_b32 v204, v181\n" \
  "v_mov_b32 v205, v181\n" \
  "s_mov_b32 %[one], 0\n" \
  "s_branch .Lfmath_%=\n" \
  ".Lfb1_%=:\n" \
  "s_mov_b32 %[st], 0\n" \
  "s_waitcnt vmcnt(0)\n" \
  "ds_read_b64_tr_b16 v[148:149], v192 offset:8192\n" \
  "ds_read_b64_tr_b16 v[150:151], v192 offset:8192\n" \
  "ds_read_b64_tr_b16 v[152:153], v193 offset:8192\n" \
  "ds_read_b64_tr_b16 v[154:155], v193 offset:8192\n" \
  "ds_read_b64_tr_b16 v[156:157], v192 offset:8448\n" \
  "ds_read_b64_tr_b16 v[158:159], v192 offset:8448\n" \
  "ds_read_b64_tr_b16 v[160:161], v193 offset:8448\n" \
  "ds_read_b64_tr_b16 v[162:163], v193 offset:8448\n" \
  "ds_read_b64_tr_b16 v[164:165], v192 offset:8704\n" \
  "ds_read_b64_tr_b16 v[166:167], v192 offset:8704\n" \
  "ds_read_b64_tr_b16 v[168:169], v193 offset:8704\n" \
  "ds_read_b64_tr_b16 v[170:171], v193 offset:8704\n" \
  "ds_read_b64_tr_b16 v[172:173], v192 offset:8960\n" \
  "ds_read_b64_tr_b16 v[174:175], v192 offset:8960\n" \
  "ds_read_b64_tr_b16 v[176:177], v193 offset:8960\n" \
  "ds_read_b64_tr_b16 v[178:179], v193 offset:8960\n" \
  "v_mov_b32 v194, v184\n" \
  "v_mov_b32 v195, v184\n" \
  "v_mov_b32 v204, v185\n" \
  "v_mov_b32 v205, v185\n" \
  "s_mov_b32 %[one], 0\n" \
  "s_branch .Lfmath_%=\n" \
  ".Lfb2_%=:\n" \
  "s_mov_b32 %[st], 0\n" \
  "s_waitcnt vmcnt(0)\n" \
  "ds_read_b64_tr_b16 v[148:149], v192 offset:8192\n" \
  "ds_read_b64_tr_b16 v[150:151], v192 offset:12288\n" \
  "ds_read_b64_tr_b16 v[152:153], v193 offset:8192\n" \
  "ds_read_b64_tr_b16 v[154:155], v193 offset:12288\n" \
  "ds_read_b64_tr_b16 v[156:157], v192 offset:8448\n" \
  "ds_read_b64_tr_b16 v[158:159], v192 offset:12544\n" \
  "ds_read_b64_tr_b16 v[160:161], v193 offset:8448\n" \
  "ds_read_b64_tr_b16 v[162:163], v193 offset:12544\n" \
  "ds_read_b64_tr_b16 v[164:165], v192 offset:8704\n" \
  "ds_read_b64_tr_b16 v[166:167], v192 offset:12800\n" \
  "ds_read_b64_tr_b16 v[168:169], v193 offset:8704\n" \
  "ds_read_b64_tr_b16 v[170:171], v193 offset:12800\n" \
  "ds_read_b64_tr_b16 v[172:173], v192 offset:8960\n" \
  "ds_read_b64_tr_b16 v[174:175], v192 offset:13056\n" \
  "ds_read_b64_tr_b16 v[176:177], v193 offset:8960\n" \
  "ds_read_b64_tr_b16 v[178:179], v193 offset:13056\n" \
  "v_mov_b32 v194, v184\n" \
  "v_mov_b32 v195, v186\n" \
  "v_mov_b32 v204, v185\n" \
  "v_mov_b32 v205, v187\n" \
  "s_mov_b32 %[one], 1.0\n" \
  "s_branch .Lfmath_%=\n" \
  ".Lfn2_%=:\n" \
  "v_add_u32 v192, %[slot], %[rdp]\n" \
  "v_xor_b32 v193, 32, v192\n" \
  "s_mov_b32 %[st], 0\n" \
  "s_waitcnt vmcnt(0)\n" \
  "ds_read_b64_tr_b16 v[148:149], v192 offset:0\n" \
  "ds_read_b64_tr_b16 v[150:151], v192 offset:4096\n" \
  "ds_read_b64_tr_b16 v[152:153], v193 offset:0\n" \
  "ds_read_b64_tr_b16 v[154:155], v193 offset:4096\n" \
  "ds_read_b64_tr_b16 v[156:157], v192 offset:256\n" \
  "ds_read_b64_tr_b16 v[158:159], v192 offset:4352\n" \
  "ds_read_b64_tr_b16 v[160:161], v193 offset:256\n" \
  "ds_read_b64_tr_b16 v[162:163], v193 offset:4352\n" \
  "ds_read_b64_tr_b16 v[164:165], v192 offset:512\n" \
  "ds_read_b64_tr_b16 v[166:167], v192 offset:4608\n" \
  "ds_read_b64_tr_b16 v[168:169], v193 offset:512\n" \
  "ds_read_b64_tr_b16 v[170:171], v193 offset:4608\n" \
  "ds_read_b64_tr_b16 v[172:173], v192 offset:768\n" \
  "ds_read_b64_tr_b16 v[174:175], v192 offset:4864\n" \
  "ds_read_b64_tr_b16 v[176:177], v193 offset:768\n" \
  "ds_read_b64_tr_b16 v[178:179], v193 offset:4864\n" \
  "v_mov_b32 v194, v180\n" \
  "v_mov_b32 v195, v182\n" \
  "v_mov_b32 v204, v181\n" \
  "v_mov_b32 v205, v183\n" \
  "s_mov_b32 %[one], 1.0\n" \
    \
  ".Lfmath_%=:\n" \
  "v_sub_f32 v188, %[nm], v194\n" \
  "v_sub_f32 v189, %[nm], v195\n" \
  "v_max_f32 v190, v188, v189\n" \
  "v_cmp_lt_f32 vcc, %[lim], v190\n" \
  "s_cbranch_vccnz .Lfresc_%=\n" \
  ".Lfw_%=:\n" \
  "v_cvt_i32_f32 v188, v188\n" \
  "v_cvt_i32_f32 v189, v189\n" \
  "v_ldexp_f32 v188, 1.0, v188\n" \
  "v_ldexp_f32 v189, %[one], v189\n" \
  "v_fmac_f32 v96, v204, v188\n" \
  "v_perm_b32 v188, v188, v188, %[sel]\n" \
  "v_fmac_f32 v96, v205, v189\n" \
  "v_perm_b32 v189, v189, v189, %[sel]\n" \
  "v_and_b32 v196, v188, %[mk0]\n" \
  "v_and_b32 v197, v188, %[mk1]\n" \
  "v_and_b32 v198, v189, %[mk0]\n" \
  "v_and_b32 v199, v189, %[mk1]\n" \
  "s_cmp_eq_u32 %[st], 1\n" \
  "s_waitcnt lgkmcnt(14)\n" \
  "s_nop 0\n" \
  "v_mfma_f32_16x16x32_bf16 v[64:67], v[148:151], v[196:199], v[64:67]\n" \
  "s_waitcnt lgkmcnt(12)\n" \
  "v_mfma_f32_16x16x32_bf16 v[68:71], v[152:155], v[196:199], v[68:71]\n" \
  "s_waitcnt lgkmcnt(10)\n" \
  "v_mfma_f32_16x16x32_bf16 v[72:75], v[156:159], v[196:199], v[72:75]\n" \
  "s_waitcnt lgkmcnt(8)\n" \
  "v_mfma_f32_16x16x32_bf16 v[76:79], v[160:163], v[196:199], v[76:79]\n" \
  "s_waitcnt lgkmcnt(6)\n" \
  "v_mfma_f32_16x16x32_bf16 v[80:83], v[164:167], v[196:199], v[80:83]\n" \
  "s_waitcnt lgkmcnt(4)\n" \
  "v_mfma_f32_16x16x32_bf16 v[84:87], v[168:171], v[196:199], v[84:87]\n" \
  "s_waitcnt lgkmcnt(2)\n" \
  "v_mfma_f32_16x16x32_bf16 v[88:91], v[172:175], v[196:199], v[88:91]\n" \
  "s_waitcnt lgkmcnt(0)\n" \
  "v_mfma_f32_16x16x32_bf16 v[92:95], v[176:179], v[196:199], v[92:95]\n" \
  "s_cbranch_scc1 .Lfb1_%=\n" \
  "s_cmp_eq_u32 %[st], 2\n" \
  "s_cbranch_scc1 .Lfb2_%=\n" \
  "s_cmp_lt_i32 %[c], %[k]\n" \
  "s_cbranch_scc1 .Lfbatch_%=\n" \
    \
  ".Lfout_%=:\n" \
  "v_rcp_f32 v188, v96\n" \
  "s_nop 0\n" \
  "v_mov_b32 v189, v188\n" \
  "v_pk_mul_f32 v[64:65], v[64:65], v[188:189]\n" \
  "v_pk_mul_f32 v[66:67], v[66:67], v[188:189]\n" \
  "v_pk_mul_f32 v[68:69], v[68:69], v[188:189]\n" \
  "v_pk_mul_f32 v[70:71], v[70:71], v[188:189]\n" \
  "v_cvt_pk_bf16_f32 v148, v64, v65\n" \
  "v_cvt_pk_bf16_f32 v150, v68, v69\n" \
  "v_cvt_pk_bf16_f32 v149, v66, v67\n" \
  "v_cvt_pk_bf16_f32 v151, v70, v71\n" \
  "s_nop 0\n" \
  "v_permlane16_swap_b32 v148, v150\n" \
  "v_permlane16_swap_b32 v149, v151\n" \
  "s_nop 0\n" \
  "buffer_store_dwordx4 v[148:151], %[ov], %[so], 0 offen offset:0 sc1\n" \
  "v_pk_mul_f32 v[72:73], v[72:73], v[188:189]\n" \
  "v_pk_mul_f32 v[74:75], v[74:75], v[188:189]\n" \
  "v_pk_mul_f32 v[76:77], v[76:77], v[188:189]\n" \
  "v_pk_mul_f32 v[78:79], v[78:79], v[188:189]\n" \
  "v_cvt_pk_bf16_f32 v152, v72, v73\n" \
  "v_cvt_pk_bf16_f32 v154, v76, v77\n" \
  "v_cvt_pk_bf16_f32 v153, v74, v75\n" \
  "v_cvt_pk_bf16_f32 v155, v78, v79\n" \
  "s_nop 0\n" \
  "v_permlane16_swap_b32 v152, v154\n" \
  "v_permlane16_swap_b32 v153, v155\n" \
  "s_nop 0\n" \
  "buffer_store_dwordx4 v[152:155], %[ov], %[so], 0 offen offset:64 sc1\n" \
  "v_pk_mul_f32 v[80:81], v[80:81], v[188:189]\n" \
  "v_pk_mul_f32 v[82:83], v[82:83], v[188:189]\n" \
  "v_pk_mul_f32 v[84:85], v[84:85], v[188:189]\n" \
  "v_pk_mul_f32 v[86:87], v[86:87], v[188:189]\n" \
  "v_cvt_pk_bf16_f32 v156, v80, v81\n" \
  "v_cvt_pk_bf16_f32 v158, v84, v85\n" \
  "v_cvt_pk_bf16_f32 v157, v82, v83\n" \
  "v_cvt_pk_bf16_f32 v159, v86, v87\n" \
  "s_nop 0\n" \
  "v_permlane16_swap_b32 v156, v158\n" \
  "v_permlane16_swap_b32 v157, v159\n" \
  "s_nop 0\n" \
  "buffer_store_dwordx4 v[156:159], %[ov], %[so], 0 offen offset:128 sc1\n" \
  "v_pk_mul_f32 v[88:89], v[88:89], v[188:189]\n" \
  "v_pk_mul_f32 v[90:91], v[90:91], v[188:189]\n" \
  "v_pk_mul_f32 v[92:93], v[92:93], v[188:189]\n" \
  "v_pk_mul_f32 v[94:95], v[94:95], v[188:189]\n" \
  "v_cvt_pk_bf16_f32 v160, v88, v89\n" \
  "v_cvt_pk_bf16_f32 v162, v92, v93\n" \
  "v_cvt_pk_bf16_f32 v161, v90, v91\n" \
  "v_cvt_pk_bf16_f32 v163, v94, v95\n" \
  "s_nop 0\n" \
  "v_permlane16_swap_b32 v160, v162\n" \
  "v_permlane16_swap_b32 v161, v163\n" \
  "s_nop 0\n" \
  "buffer_store_dwordx4 v[160:163], %[ov], %[so], 0 offen offset:192 sc1\n" \
  "buffer_store_dwordx2 v[200:201], %[fo], %[sp], 0 offen offset:136 sc1\n" \
  "s_branch .Lfend_%=\n" \
    \
  ".Lfresc_%=:\n" \
  "v_max_f32 v190, 0, v190\n" \
  "v_exp_f32 v190, -v190\n" \
  "v_min3_f32 %[nm], %[nm], v194, v195\n" \
  "s_nop 7\n" \
  "v_mul_f32 v64, v190, v64\n" \
  "v_mul_f32 v65, v190, v65\n" \
  "v_mul_f32 v66, v190, v66\n" \
  "v_mul_f32 v67, v190, v67\n" \
  "v_mul_f32 v68, v190, v68\n" \
  "v_mul_f32 v69, v190, v69\n" \
  "v_mul_f32 v70, v190, v70\n" \
  "v_mul_f32 v71, v190, v71\n" \
  "v_mul_f32 v72, v190, v72\n" \
  "v_mul_f32 v73, v190, v73\n" \
  "v_mul_f32 v74, v190, v74\n" \
  "v_mul_f32 v75, v190, v75\n" \
  "v_mul_f32 v76, v190, v76\n" \
  "v_mul_f32 v77, v190, v77\n" \
  "v_mul_f32 v78, v190, v78\n" \
  "v_mul_f32 v79, v190, v79\n" \
  "v_mul_f32 v80, v190, v80\n" \
  "v_mul_f32 v81, v190, v81\n" \
  "v_mul_f32 v82, v190, v82\n" \
  "v_mul_f32 v83, v190, v83\n" \
  "v_mul_f32 v84, v190, v84\n" \
  "v_mul_f32 v85, v190, v85\n" \
  "v_mul_f32 v86, v190, v86\n" \
  "v_mul_f32 v87, v190, v87\n" \
  "v_mul_f32 v88, v190, v88\n" \
  "v_mul_f32 v89, v190, v89\n" \
  "v_mul_f32 v90, v190, v90\n" \
  "v_mul_f32 v91, v190, v91\n" \
  "v_mul_f32 v92, v190, v92\n" \
  "v_mul_f32 v93, v190, v93\n" \
  "v_mul_f32 v94, v190, v94\n" \
  "v_mul_f32 v95, v190, v95\n" \
  "v_mul_f32 v96, v190, v96\n" \
  "v_sub_f32 v188, %[nm], v194\n" \
  "v_sub_f32 v189, %[nm], v195\n" \
  "s_branch .Lfw_%=\n" \
  ".Lfend_%=:\n" \
  ".Lattn_last:\n" \
  "s_nop 1\n"

__global__ void __launch_bounds__(320, 2)
attn_paged(const __bf16* __restrict__ q, const __bf16* __restrict__ kc, const __bf16* __restrict__ vc,
           const int* __restrict__ bt, const int* __restrict__ lens, __bf16* __restrict__ out, char* ws, unsigned kv_bytes, unsigned unused) {
  asm volatile(LOADER_ASM
               :: [tid] "v"(threadIdx.x), [ka] "s"(__builtin_amdgcn_kernarg_segment_ptr()), [wg] "s"(blockIdx.x),
                  [ctl] "i"(CTL_OFF), [done] "i"(DONE_OFF), [bt] "i"(BT_OFF), [junk] "i"(JUNK_OFF + 2048), [xc] "i"(XC),
                  [tau] "i"(TAU), [lead] "i"(LEAD), [cold] "i"(COLD), [slow] "i"(SLOW), [cap] "i"(SPIN_CAP),
                  [rep] "i"(PART_TOTAL), [fl] "i"(FB_MIN), [fa] "i"(FB_ADD)
               : "memory", "scc", "vcc", LOADER_CLOB);
  const int lane = threadIdx.x & 63;
  const int w = __builtin_amdgcn_readfirstlane(threadIdx.x >> 6);
  const int g = lane >> 4, i = lane & 15;
  const int h = blockIdx.x & 7, idx = blockIdx.x >> 3;
  const bool owner = idx < 16;
  asm volatile("s_barrier" ::: "memory");
  const int rawlen = ((const GAS int*)lens)[i];
  const __amdgpu_buffer_rsrc_t srdP = __builtin_amdgcn_make_buffer_rsrc((void*)ws, (short)0, PART_TOTAL, 0x00020000);
  const __amdgpu_buffer_rsrc_t srdO = __builtin_amdgcn_make_buffer_rsrc((void*)out, (short)0, 16 * HQ * SQ * D_ * 2, 0x00020000);

  const int xg = (g >> 1) ^ (3 * (g & 1));
  const GAS __bf16* qbase = (const GAS __bf16*)q + ((size_t)(h * 4 + w) * SQ + i) * D_ + 8 * xg;
  u32x4 qn[4];
  if (owner) {
#pragma unroll
    for (int s = 0; s < 4; ++s) qn[s] = *(const GAS u32x4*)(qbase + (size_t)idx * HQ * SQ * D_ + 32 * s);
  } else if (w < 2) {
    const char* base = (idx & 1) ? (const char*)vc : (const char*)kc;
    const __amdgpu_buffer_rsrc_t srdX = __builtin_amdgcn_make_buffer_rsrc((void*)(base + h * 256), (short)0, kv_bytes - h * 256, 0x00020000);
    const unsigned so = ((((unsigned)idx * 2654435761u) >> 20) + w) << 15;
    asm volatile("s_mov_b32 m0, %0\n\ts_nop 0\n\t"
                 "buffer_load_dwordx4 %1, %2, %3 offen lds\n\tbuffer_load_dwordx4 %1, %2, %4 offen lds\n\t"
                 "buffer_load_dwordx4 %1, %2, %5 offen lds\n\tbuffer_load_dwordx4 %1, %2, %6 offen lds"
                 :: "s"(JUNK_OFF + w * 1024), "v"(g * 2048 + i * 16), "s"(srdX), "s"(so), "s"(so + 8192), "s"(so + 16384), "s"(so + 24576) : "memory");
  }
  const unsigned lm1 = min((unsigned)rawlen - 1u, (unsigned)SKV - 1u);
  const Part pt = partition(lm1, idx, i);
  if (pt.segmask == 0) return;
  const int ntot = rl(row_scan(pt.scnt), 15);
  const unsigned rdK = (i >> 2) * 1024 + (i & 3) * 64 + 16 * ((i >> 2) ^ xg);
  const unsigned rdV = 8192 + g * 1024 + (i >> 2) * 64 + 32 * (g & 1) + 8 * (i & 3);
  const unsigned doneaddr = DONE_OFF + 4 * w, ctladdr = CTL_OFF;
  if (!owner) {
    __builtin_amdgcn_s_sleep(4);
    asm volatile("" ::: "memory");
#pragma unroll
    for (int s = 0; s < 4; ++s) qn[s] = *(const GAS u32x4*)(qbase + (size_t)__builtin_ctz(pt.segmask) * HQ * SQ * D_ + 32 * s);
  }
  const int bl = 31 - __builtin_clz(pt.segmask);
  const unsigned hm = helpers_of(pt, bl, i);
  int np = __builtin_popcount(hm);
  const int fi = (w * min(np, 4)) >> 2;
  const bool fin = owner ? fi == 0 : (fi > 0 && __builtin_popcount(hm & ((2u << (idx - 16)) - 1u)) == fi);
  const unsigned rbase = h * 48 * WGREC + w * REC;
  unsigned foff;
  {
    const int rank = __builtin_popcount(hm & ((1u << i) - 1u));
    const unsigned mine = rbase + ((!owner && i == idx - 16) ? 32 + bl : i + bl) * WGREC + 4096;
    const int got = __builtin_amdgcn_ds_permute(4 * ((lane < 16 && ((hm >> i) & 1)) ? rank : 63), (int)mine);
    foff = (fin && lane < np) ? (unsigned)got : PART_TOTAL;
  }
  const unsigned ovoff = ((bl * HQ + h * 4 + w) * SQ) * D_ * 2 + i * 256 + (g >> 1) * 16 + (g & 1) * 32;
  const unsigned hrec = rbase + (owner ? 32 : idx - 16) * WGREC;
  const unsigned flagoff = (lane < 16 && pt.scnt > 0 && !(fin && i == bl)) ? hrec + i * WGREC + 4096 + 128 : PART_TOTAL;
  asm volatile("" : "+s"(np), "+v"(foff) :: "memory");
  u32x2 epoch = {0u, 0u};

  u32x4 pk[2][4];
  u32x2 pml[2];
  unsigned prec[2] = {PART_TOTAL, PART_TOTAL};
#pragma unroll
  for (int e = 0; e < 4; ++e) pk[0][e] = pk[1][e] = u32x4{0u, 0u, 0u, 0u};
  pml[0] = pml[1] = u32x2{0u, 0u};
  int tg = 0;
  int full = 0;
  unsigned slot = 0;
  int kready = 0, spin = SPIN_CAP;
  bool touched = false;
  u32x32 kreg;
#pragma unroll
  for (int e = 0; e < 32; ++e) kreg[e] = 0u;
  unsigned rdP = rdV - 8192, mla = i * 8;
  unsigned mk0 = g == (i >> 2) ? ((i & 1) ? 0xffff0000u : 0x0000ffffu) : 0u, mk1 = (i & 2) ? mk0 : 0u;
  mk0 = (i & 2) ? 0u : mk0;
  asm volatile("" : "+v"(rdP), "+v"(mla), "+v"(mk0), "+v"(mk1));

  f32x32 acc;
  f32x4 lacc;
  float nm, thr;
  int b;
  auto pack = [&](u32x4 (&o)[4]) __attribute__((always_inline)) {
#pragma unroll
    for (int e = 0; e < 8; e += 2) {
      auto s0 = __builtin_amdgcn_permlane16_swap(cvtpk(acc[4 * e], acc[4 * e + 1]), cvtpk(acc[4 * e + 4], acc[4 * e + 5]), false, false);
      auto s1 = __builtin_amdgcn_permlane16_swap(cvtpk(acc[4 * e + 2], acc[4 * e + 3]), cvtpk(acc[4 * e + 6], acc[4 * e + 7]), false, false);
      o[e >> 1] = u32x4{s0[0], s1[0], s0[1], s1[1]};
    }
  };
  const unsigned pvoff = (i >> 2) * 1024 + (i & 3) * 64 + 16 * ((2 * (g & 1) + (g >> 1)) ^ (2 * ((i >> 2) & 1)));
  const unsigned mloff = lane < 16 ? 4096 + lane * 8 : PART_TOTAL;
  auto publish = [&](const u32x4 (&o)[4], u32x2 ml, unsigned rec) __attribute__((always_inline)) {
    unsigned a0 = rec + pvoff, a1 = rec + mloff;
    asm volatile("" : "+v"(a0), "+v"(a1));
#pragma unroll
    for (int e = 0; e < 4; ++e) __builtin_amdgcn_raw_buffer_store_b128(o[e], srdP, a0 + e * 256, 0, 16);
    __builtin_amdgcn_raw_buffer_store_b64(ml, srdP, a1, 0, 16);
  };

  for (unsigned m = pt.segmask;;) {
    b = __builtin_ctz(m);
    m &= m - 1;
    const int first = rl(pt.sfirst, b), cnt = rl(pt.scnt, b), len = rl(lm1, b) + 1;
    const int rawb = rl(rawlen, b);
    u32x16 qf;
#pragma unroll
    for (int e = 0; e < 16; ++e) qf[e] = qn[e >> 2][e & 3];
    asm volatile("" : "+{v[100:115]}"(qf));
    if (m) {
#pragma unroll
      for (int s = 0; s < 4; ++s) qn[s] = *(const GAS u32x4*)(qbase + (size_t)__builtin_ctz(m) * HQ * SQ * D_ + 32 * s);
    } else {
      epoch = __builtin_amdgcn_raw_buffer_load_b64(srdP, flagoff + 8, 0, 16);
    }
#pragma unroll
    for (int e = 0; e < 32; ++e) acc[e] = 0.f;
    lacc = f32x4{0.f, 0.f, 0.f, 0.f};
    nm = 1e30f; thr = -1e30f;
    const int mask_at = rawb <= 0 ? 0 : tg + ((len >> 5) - first);
    const int limb = len - 32 * (first - tg);
    const int segend = tg + cnt;
    do {
      int stop = segend;
      if (prec[1] != PART_TOTAL) {
        if (tg >= ntot - 1) {
          publish(pk[1], pml[1], prec[1]);
          if (prec[0] != PART_TOTAL) publish(pk[0], pml[0], prec[0]);
          prec[0] = prec[1] = PART_TOTAL;
        } else stop = min(stop, ntot - 1);
      }
      if (!m && w == 0 && !touched) {
        if (tg >= ntot - TOUCH) {
          u64 pc;
          unsigned co;
          asm volatile("s_mov_b32 m0, %2\n\ts_getpc_b64 %0\n"
                       ".Ltouch_%=:\n\t"
                       "buffer_load_dword %3, %4, 0 offen sc1 lds\n\tbuffer_load_dword %5, %6, 0 offen sc1 lds\n\t"
                       "v_min_u32 %1, .Lattn_last-.Ltouch_%=, %7\n\t"
                       "global_load_lds_dword %1, %0"
                       : "=&s"(pc), "=&v"(co)
                       : "s"(JUNK_OFF), "v"(lane ? 0x7fffffffu : ovoff), "s"(srdO), "v"(lane ? 0x7fffffffu : hrec + b * WGREC + 4096 + 192), "s"(srdP),
                         "v"(lane * 128) : "memory");
          touched = true;
        } else stop = min(stop, ntot - TOUCH);
      }
      unsigned nv, tmp, vd;
      asm volatile(TILE_LOOP_ASM
                   : "+{v[64:95]}"(acc), "+{v[96:99]}"(lacc), "+{v[116:147]}"(kreg), [nm] "+v"(nm), [thr] "+v"(thr),
                     [tg] "+s"(tg), [full] "+s"(full), [kr] "+s"(kready), [spin] "+s"(spin), [slot] "+s"(slot),
                     [nv] "=&s"(nv), [tmp] "=&s"(tmp), [vd] "=&s"(vd)
                   : "{v[100:115]}"(qf), [rdk] "v"(rdK), [rdv] "v"(rdV), [ctl] "v"(ctladdr), [done] "v"(doneaddr), [g4] "v"(4 * g),
                     [stop] "s"(stop), [maskat] "s"(mask_at), [limb] "s"(limb), [zq] "s"(rawb), [c] "s"(SCALE_LOG2E), [ic] "s"(1.0f / SCALE_LOG2E)
                   : "memory", "vcc", "scc", TILE_CLOB);
    } while (tg < segend);
    if (!m) break;
    if (prec[0] != PART_TOTAL) publish(pk[0], pml[0], prec[0]);
#pragma unroll
    for (int e = 0; e < 4; ++e) pk[0][e] = pk[1][e];
    pml[0] = pml[1]; prec[0] = prec[1];
    pack(pk[1]);
    pml[1] = u32x2{__float_as_uint(nm), __float_as_uint(lacc[0])};
    prec[1] = hrec + b * WGREC;
  }
  if (!fin) {
    u32x4 o[4];
    pack(o);
    publish(o, u32x2{__float_as_uint(nm), __float_as_uint(lacc[0])}, hrec + b * WGREC);
  }
  {
    const u64 t = (((u64)epoch[1] << 32) | epoch[0]) + TAG_STEP;
    asm volatile("s_waitcnt vmcnt(0)" ::: "memory");
    __builtin_amdgcn_raw_buffer_store_b64(u32x2{(unsigned)t, (unsigned)(t >> 32)}, srdP, flagoff, 0, 16);
  }
  if (fin) {
    for (;;) {
      const u32x4 dn = *(volatile LAS u32x4*)(size_t)(unsigned)DONE_OFF;
      if (__builtin_amdgcn_readfirstlane((int)min(min(dn[0], dn[1]), min(dn[2], dn[3]))) >= ntot - 4 || --spin < 0) break;
    }
    slot = (slot + w * SLOT_BYTES) & (NSLOT * SLOT_BYTES - 1);
    unsigned need, one, tmp, t2, c, st;
    asm volatile(FINISH_ASM
                 : "+{v[64:95]}"(acc), "+{v[96:99]}"(lacc), [nm] "+v"(nm), [spin] "+s"(spin),
                   [need] "=&s"(need), [one] "=&s"(one), [tmp] "=&s"(tmp), [t2] "=&s"(t2), [c] "=&s"(c), [st] "=&s"(st)
                 : [k] "s"(np), [slot] "s"(slot), [fo] "v"(foff), [rdp] "v"(rdP), [i8] "v"(mla), [l16] "v"(lane * 16), [mk0] "v"(mk0), [mk1] "v"(mk1),
                   [ov] "v"(ovoff), [so] "s"(srdO), [sp] "s"(srdP), [lim] "s"(8.0f), [sel] "s"(0x03020302u),
                   [tlo] "i"((int)(unsigned)TAG_STEP), [thi] "i"((int)(unsigned)(TAG_STEP >> 32))
                 : "memory", "vcc", "scc", TILE_CLOB);
  }
}

extern "C" void kernel_launch(void* const* d_in, const int* in_sizes, int n_in, void* d_out, int out_size, void* d_ws,
                              size_t ws_size, hipStream_t stream) {
  const unsigned kv_bytes = (unsigned)in_sizes[1] * 2u;
  (void)hipFuncSetAttribute((const void*)attn_paged, hipFuncAttributeMaxDynamicSharedMemorySize, LDS_BYTES);
  attn_paged<<<256, 320, LDS_BYTES, stream>>>((const __bf16*)d_in[0], (const __bf16*)d_in[1], (const __bf16*)d_in[2],
                                              (const int*)d_in[3], (const int*)d_in[4], (__bf16*)d_out, (char*)d_ws, kv_bytes, 0u);
}
```
